```python
import torch

def _softmax(x, axis=-1):
    return torch.softmax(x, dim=axis)
def _gelu(x):
    return torch.nn.functional.gelu(x, approximate="tanh")
def _silu(x):
    return torch.nn.functional.silu(x)
def _sigmoid(x):
    return torch.sigmoid(x)
def _layernorm(x, axis=-1, eps=1e-5):
    m = x.mean(dim=axis, keepdim=True)
    v = x.var(dim=axis, keepdim=True, unbiased=False)
    return (x - m) / torch.sqrt(v + eps)
def _scatter_add(tgt, idx, src, axis=0):
    return tgt.clone().index_add_(axis, idx, src)
def _segment_sum(src, seg, n):
    out = torch.zeros((n,) + src.shape[1:], dtype=src.dtype)
    return out.index_add_(0, seg, src)
def _pad(x, pw):
    flat = tuple(v for pair in reversed(pw) for v in pair)
    return torch.nn.functional.pad(x, flat)

kernel_name = 'gated_mix__ssm_conv_h3__dense_activation_silu__prompt'

def setup_inputs():

    _w = (lambda a: (lambda t: t.to(torch.bfloat16) if t.is_floating_point() else t)(torch.as_tensor(a)))
    def _A_setup():
        B, S, D = 1, 2048, 512
        ks = 4
        return {
            "q": _w((lambda *s: torch.randn(*s, dtype=torch.bfloat16) * 0.05)(B, S, D)),
            "k": _w((lambda *s: torch.randn(*s, dtype=torch.bfloat16) * 0.2)(B, S, D)),
            "v": _w((lambda *s: torch.randn(*s, dtype=torch.bfloat16) * 0.2)(B, S, D)),
            "shift_k": _w((lambda *s: torch.randn(*s, dtype=torch.bfloat16) * 0.2)(D, ks) * 0.5),
            "ssm_k": _w((lambda *s: torch.randn(*s, dtype=torch.bfloat16) * 0.4)(D, S) * (1.0 / S ** 0.5)),
            "ssm_D": _w((lambda *s: torch.randn(*s, dtype=torch.bfloat16) * 0.2)(D)),
        }
    _A_in = _A_setup()

    def _B_setup():
        B, S, D, F = 1, 2048, 512, 1792
        return {
            "x": _w((lambda *s: torch.randn(*s, dtype=torch.bfloat16) * 0.05)(B, S, D)),
            "w1": _w((lambda *s: torch.randn(*s, dtype=torch.bfloat16) * 3.2)(D, F) * (D ** -0.5)),
            "w2": _w((lambda *s: torch.randn(*s, dtype=torch.bfloat16) * 0.05)(F, D) * (F ** -0.5)),
        }
    _B_in = _B_setup()

    return {
        "A_q": _A_in["q"],
        "A_k": _A_in["k"],
        "A_v": _A_in["v"],
        "A_shift_k": _A_in["shift_k"],
        "A_ssm_k": _A_in["ssm_k"],
        "A_ssm_D": _A_in["ssm_D"],
        "B_x": _B_in["x"],
        "B_w1": _B_in["w1"],
        "B_w2": _B_in["w2"],
        "gate_w": _w((lambda *s: torch.randn(*s, dtype=torch.bfloat16) * 0.05)(512) * 16.0),
    }

def reference(A_q, A_k, A_v, A_shift_k, A_ssm_k, A_ssm_D, B_x, B_w1, B_w2, gate_w):
    def _A_ref(q, k, v, shift_k, ssm_k, ssm_D):
        B, S, D = q.shape
        ks = shift_k.shape[-1]
        k_pad = _pad(k, ((0, 0), (ks - 1, 0), (0, 0)))
        k_shift = torch.zeros_like(k)
        for t in range(ks):
            k_shift = k_shift + k_pad[:, t:t + S, :] * shift_k[:, ks - 1 - t][None, None, :]
        kv = k_shift * v
        L = 2 * S
        kv_pad = _pad(kv, ((0, 0), (0, S), (0, 0)))
        kf_pad = _pad(ssm_k, ((0, 0), (0, S)))
        kv_f = (lambda x, axes=(-2,-1): torch.fft.rfft2(x.float(), dim=axes))(kv_pad[:, None, :, :], axes=(1, 2))
        kf_f = (lambda x, axes=(-2,-1): torch.fft.rfft2(x.float(), dim=axes))(kf_pad[:, None, :], axes=(1, 2))
        kf_f = (lambda x, *d: x.permute(*d))(kf_f, 1, 2, 0)
        y_f = kv_f * kf_f[None, :, :, :]
        y = (lambda x, s, axes=(-2,-1): torch.fft.irfft2(x, s=s, dim=axes).to(torch.bfloat16))(y_f, (1, L), axes=(1, 2))[:, 0, :S, :]
        y = y + kv * ssm_D[None, None, :]
        return q * y

    def _B_ref(x, w1, w2):
        h = torch.einsum("bsd,df->bsf", x, w1)
        h = _silu(h)
        out = torch.einsum("bsf,fd->bsd", h, w2)
        return out

    x = A_q
    g = _sigmoid((x * gate_w).sum(-1, keepdims=True))
    a = _A_ref(x, A_k, A_v, A_shift_k, A_ssm_k, A_ssm_D)
    b = _B_ref(x, B_w1, B_w2)
    return g * a + (1 - g) * b
```

```cpp
#include <hip/hip_runtime.h>
#include <hip/hip_bf16.h>
#include <stdint.h>
typedef unsigned short u16;
using bf16x8 = __attribute__((ext_vector_type(8))) short;
using f32x4 = __attribute__((ext_vector_type(4))) float;
constexpr int S = 2048, D = 512, F = 1792;

__device__ __forceinline__ float bf2f(u16 x) { return __uint_as_float(((unsigned)x) << 16); }
__device__ __forceinline__ u16 f2bf(float f) { __hip_bfloat16 b = __float2bfloat16(f); return *(u16*)&b; }
__device__ __forceinline__ float rbf(float f) { return bf2f(f2bf(f)); }

__global__ void prep_k(const u16* k, const u16* v, const u16* sk, const u16* q, const u16* gw, u16* kvT, float* g) {
    int idx = blockIdx.x * blockDim.x + threadIdx.x;
    int s = idx / D, d = idx % D;
    float acc = 0.f;
    for (int t = 0; t < 4; ++t) {
        int ss = s + t - 3;
        float kk = ss >= 0 ? bf2f(k[ss * D + d]) : 0.f;
        acc = rbf(acc + rbf(kk * bf2f(sk[d * 4 + 3 - t])));
    }
    kvT[d * S + s] = f2bf(acc * bf2f(v[s * D + d]));
    if (idx < S) {
        float sum = 0.f;
        for (int j = 0; j < D; ++j) sum += rbf(bf2f(q[idx * D + j]) * bf2f(gw[j]));
        sum = rbf(sum);
        g[idx] = rbf(1.f / (1.f + __expf(-sum)));
    }
}
__global__ void conv_k(const u16* kvT, const u16* hk, const u16* Dv, u16* zT) {
    __shared__ float u[S], h[S];
    int d = blockIdx.x;
    for (int i = threadIdx.x; i < S; i += 256) { u[i] = bf2f(kvT[d * S + i]); h[i] = bf2f(hk[d * S + i]); }
    __syncthreads();
    float dd = bf2f(Dv[d]);
    for (int s = threadIdx.x; s < S; s += 256) {
        float acc = 0.f;
        for (int j = 0; j <= s; ++j) acc += h[j] * u[s - j];
        zT[d * S + s] = f2bf(rbf(acc) + rbf(u[s] * dd));
    }
}
template <int MODE>
__global__ void gemm_k(const u16* A, const u16* B, u16* C, int M, int N, int K, const u16* zT, const u16* q, const float* g) {
    __shared__ u16 As[64][40], Bs[64][40];
    int bm = blockIdx.y * 64, bn = blockIdx.x * 64;
    int tid = threadIdx.x, wid = tid >> 6, lane = tid & 63;
    int wr = wid >> 1, wc = wid & 1;
    f32x4 acc[2][2] = {};
    for (int k0 = 0; k0 < K; k0 += 32) {
        for (int i = tid; i < 64 * 32; i += 256) {
            int r = i / 32, c = i % 32;
            As[r][c] = A[(bm + r) * K + k0 + c];
            int kk = i / 64, n = i % 64;
            Bs[n][kk] = B[(k0 + kk) * N + bn + n];
        }
        __syncthreads();
        for (int m = 0; m < 2; ++m) for (int n = 0; n < 2; ++n) {
            bf16x8 a = *(const bf16x8*)&As[wr * 32 + m * 16 + (lane & 15)][(lane >> 4) * 8];
            bf16x8 b = *(const bf16x8*)&Bs[wc * 32 + n * 16 + (lane & 15)][(lane >> 4) * 8];
            acc[m][n] = __builtin_amdgcn_mfma_f32_16x16x32_bf16(a, b, acc[m][n], 0, 0, 0);
        }
        __syncthreads();
    }
    for (int m = 0; m < 2; ++m) for (int n = 0; n < 2; ++n) for (int j = 0; j < 4; ++j) {
        int r = bm + wr * 32 + m * 16 + (lane >> 4) * 4 + j, c = bn + wc * 32 + n * 16 + (lane & 15);
        float x = rbf(acc[m][n][j]);
        if (MODE == 0) {
            C[r * N + c] = f2bf(x / (1.f + __expf(-x)));
        } else {
            float gg = g[r];
            float a = rbf(bf2f(q[r * D + c]) * bf2f(zT[c * S + r]));
            C[r * N + c] = f2bf(rbf(gg * a) + rbf(rbf(1.f - gg) * x));
        }
    }
}
extern "C" void kernel_launch(void* const* d_in, const int* in_sizes, int n_in,
                              void* d_out, int out_size, void* d_ws, size_t ws_size,
                              hipStream_t stream) {
    const u16* q = (const u16*)d_in[0]; const u16* k = (const u16*)d_in[1]; const u16* v = (const u16*)d_in[2];
    const u16* sk = (const u16*)d_in[3]; const u16* hk = (const u16*)d_in[4]; const u16* Dv = (const u16*)d_in[5];
    const u16* w1 = (const u16*)d_in[7]; const u16* w2 = (const u16*)d_in[8]; const u16* gw = (const u16*)d_in[9];
    char* ws = (char*)d_ws;
    u16* kvT = (u16*)ws; u16* zT = (u16*)(ws + (2 << 20)); float* g = (float*)(ws + (4 << 20)); u16* h = (u16*)(ws + (5 << 20));
    prep_k<<<S * D / 256, 256, 0, stream>>>(k, v, sk, q, gw, kvT, g);
    conv_k<<<D, 256, 0, stream>>>(kvT, hk, Dv, zT);
    gemm_k<0><<<dim3(F / 64, S / 64), 256, 0, stream>>>(q, w1, h, S, F, D, nullptr, nullptr, nullptr);
    gemm_k<1><<<dim3(D / 64, S / 64), 256, 0, stream>>>(h, w2, (u16*)d_out, S, D, F, zT, q, g);
}
```

```cpp
#include <hip/hip_runtime.h>
#include <hip/hip_bf16.h>
#include <stdint.h>

#define LAS __attribute__((address_space(3)))
#define GAS __attribute__((address_space(1)))
typedef unsigned short u16;
typedef short s16x4 __attribute__((ext_vector_type(4)));
typedef short s16x8 __attribute__((ext_vector_type(8)));
typedef float f32x4 __attribute__((ext_vector_type(4)));
typedef unsigned v4u __attribute__((ext_vector_type(4)));
typedef unsigned v2u __attribute__((ext_vector_type(2)));
typedef __bf16 bf16x2_t __attribute__((ext_vector_type(2)));
typedef float f32x2 __attribute__((ext_vector_type(2)));

constexpr int S = 2048, D = 512, F = 1792;
constexpr unsigned SENT = 0xFFFFFFFFu;

constexpr size_t WS_HF = 0;
constexpr size_t WS_W2F = 8u << 20;
constexpr size_t WS_UT = 10u << 20;
constexpr size_t WS_ZT = 12u << 20;
constexpr size_t WS_G = 14u << 20;

__device__ __forceinline__ float bflo(unsigned w) { return __uint_as_float(w << 16); }
__device__ __forceinline__ float bfhi(unsigned w) { return __uint_as_float(w & 0xFFFF0000u); }
__device__ __forceinline__ unsigned pk2(float lo, float hi) {
    f32x2 f = {lo, hi};
    bf16x2_t b = __builtin_convertvector(f, bf16x2_t);
    return __builtin_bit_cast(unsigned, b);
}
__device__ __forceinline__ float rbf(float f) { return bflo(pk2(f, f)); }
__device__ __forceinline__ s16x4 trread(LAS char* p) { return __builtin_amdgcn_ds_read_tr16_b64_v4i16((LAS s16x4*)p); }
__device__ __forceinline__ s16x8 cat8(s16x4 a, s16x4 b) { return __builtin_shufflevector(a, b, 0, 1, 2, 3, 4, 5, 6, 7); }
__device__ __forceinline__ void glds16(const void* base, unsigned voff, LAS char* l) {
    unsigned keep;
    asm volatile("s_mov_b32 %0, m0\n\ts_mov_b32 m0, %3\n\ts_nop 0\n\tglobal_load_lds_dwordx4 %1, %2\n\ts_mov_b32 m0, %0"
                 : "=&s"(keep) : "v"(voff), "s"(base), "s"((unsigned)(size_t)l) : "memory");
}
#define WAIT_V(n) asm volatile("s_waitcnt vmcnt(" #n ")" ::: "memory")
#define WAIT_L0() asm volatile("s_waitcnt lgkmcnt(0)" ::: "memory")
#define BAR() asm volatile("s_barrier" ::: "memory")
#define SCHED() __builtin_amdgcn_sched_barrier(0)
#define MFMA(a, b, c) __builtin_amdgcn_mfma_f32_16x16x32_bf16(a, b, c, 0, 0, 0)

__device__ __forceinline__ float silu(float x) {
    float e = __builtin_amdgcn_exp2f(-1.44269504088896f * x);
    return x * __builtin_amdgcn_rcpf(1.0f + e);
}

constexpr int K1_KV = 131072;
constexpr int K1_LDS = K1_KV + 18432;

__device__ __forceinline__ void kv_prep(LAS char* sm, int w, int lane, int rt, int cg, u16* uT) {
    const int gq = lane >> 4, qq = (lane & 15) >> 2, p = lane & 3;
    LAS char* kb = sm + K1_KV + (8 * w + 4 + qq) * 128 + 32 * gq + 8 * p;
    v2u k0 = __builtin_bit_cast(v2u, trread(kb));
    v2u k1 = __builtin_bit_cast(v2u, trread(kb + 512));
    v2u k2 = __builtin_bit_cast(v2u, trread(kb + 1024));
    LAS char* vb = sm + K1_KV + 9216 + (8 * w + qq) * 128 + 32 * gq + 8 * p;
    v2u v0 = __builtin_bit_cast(v2u, trread(vb));
    v2u v1 = __builtin_bit_cast(v2u, trread(vb + 512));
    v2u sk = *(LAS v2u*)(sm + K1_KV + 17408 + 8 * lane);
    if (rt == 0 && w == 0) { k0.x = 0; k0.y = 0; }
    float kk[12] = {bflo(k0.x), bfhi(k0.x), bflo(k0.y), bfhi(k0.y), bflo(k1.x), bfhi(k1.x), bflo(k1.y), bfhi(k1.y),
                    bflo(k2.x), bfhi(k2.x), bflo(k2.y), bfhi(k2.y)};
    float vv[8] = {bflo(v0.x), bfhi(v0.x), bflo(v0.y), bfhi(v0.y), bflo(v1.x), bfhi(v1.x), bflo(v1.y), bfhi(v1.y)};
    const float w0 = bflo(sk.x), w1 = bfhi(sk.x), w2 = bflo(sk.y), w3 = bfhi(sk.y);
    float ks[8];
#pragma unroll
    for (int i = 0; i < 8; ++i) ks[i] = kk[i + 1] * w3 + kk[i + 2] * w2 + kk[i + 3] * w1 + kk[i + 4] * w0;
    float u[8];
#pragma unroll
    for (int i = 0; i < 8; i += 2) {
        unsigned r = pk2(ks[i], ks[i + 1]);
        u[i] = bflo(r) * vv[i];
        u[i + 1] = bfhi(r) * vv[i + 1];
    }
    v4u o = {pk2(u[7], u[6]), pk2(u[5], u[4]), pk2(u[3], u[2]), pk2(u[1], u[0])};
    const int s0 = 64 * rt + 8 * w;
    *(GAS v4u*)(uT + (size_t)(64 * cg + lane) * S + (2040 - s0)) = o;
}

__global__ __launch_bounds__(512, 2) void k1(const u16* __restrict__ q, const u16* __restrict__ kin, const u16* __restrict__ vin,
                                             const u16* __restrict__ sk, const u16* __restrict__ w1, const u16* __restrict__ w2,
                                             const u16* __restrict__ gw, char* __restrict__ ws) {
    __shared__ __attribute__((aligned(16))) char smem[K1_LDS];
    LAS char* sm = (LAS char*)smem;
    const int b = blockIdx.x, x = b & 7, idx = b >> 3;
    const int tid = threadIdx.x, lane = tid & 63;
    const int w = __builtin_amdgcn_readfirstlane(tid >> 6);
    u16* uT = (u16*)(ws + WS_UT);

    {
        const int r = 64 * idx + 8 * w + (lane >> 3);
        const unsigned off = (unsigned)(r * D + 64 * x + (lane & 7) * 8) * 2u;
        glds16(kin, off, sm + K1_KV + 1024 * (w + 1));
        glds16(vin, off, sm + K1_KV + 9216 + 1024 * w);
        if (w == 0) {
            int rr = 64 * idx - 8 + (lane >> 3);
            rr = rr < 0 ? 0 : rr;
            glds16(kin, (unsigned)(rr * D + 64 * x + (lane & 7) * 8) * 2u, sm + K1_KV);
        } else if (w == 1) {
            glds16(sk, (unsigned)(256 * x + (lane & 31) * 8) * 2u, sm + K1_KV + 17408);
        }
    }
    if (idx < 28) {
        const int rp = 4 * (x >> 1) + (idx & 3), fp = 7 * (x & 1) + (idx >> 2);
        const int rot = (2 * (idx & 3) + (idx >> 2)) & 7;
        const unsigned xs0 = (unsigned)((rp * 128 + 16 * w + (lane >> 3)) * D + (((lane & 7) ^ ((lane >> 4) & 7)) * 8)) * 2u;
        const unsigned xs1 = (unsigned)((rp * 128 + 16 * w + 8 + (lane >> 3)) * D + (((lane & 7) ^ ((4 + (lane >> 4)) & 7)) * 8)) * 2u;
        const int keyw = (lane >> 4) | ((w & 1) << 2);
        const unsigned ws0 = (unsigned)((8 * w + (lane >> 4)) * F + fp * 128 + ((((((lane & 15) >> 1) ^ keyw) << 1) | (lane & 1)) * 8)) * 2u;
#define STAGE(c)                                                          \
    do {                                                                  \
        const int kc = ((c) + rot) & 7;                                   \
        LAS char* sl = sm + ((c) & 3) * 32768;                            \
        glds16(q + kc * 64, xs0, sl + 2048 * w);                          \
        glds16(q + kc * 64, xs1, sl + 2048 * w + 1024);                   \
        glds16(w1 + kc * 64 * F, ws0, sl + 16384 + 2048 * w);             \
        glds16(w1 + kc * 64 * F + 4 * F, ws0, sl + 16384 + 2048 * w + 1024); \
    } while (0)
        STAGE(0); STAGE(1); STAGE(2); STAGE(3);
        const int sw = w & 3, fh = w >> 2;
        const int c16 = lane & 15, qd = lane >> 4, qq = c16 >> 2, p = lane & 3;
        const int boff0 = (32 * sw + c16) * 128 + 16 * ((qd) ^ (c16 >> 1));
        const int boff1 = (32 * sw + c16) * 128 + 16 * ((4 + qd) ^ (c16 >> 1));
        const int key = qq | ((qd & 1) << 2);
        int aoff[4];
#pragma unroll
        for (int ft = 0; ft < 4; ++ft) aoff[ft] = 16384 + (8 * qd + qq) * 256 + 32 * ((4 * fh + ft) ^ key) + 8 * p;
        f32x4 acc[4][2] = {};
#define COMPUTE(c)                                                                                  \
    do {                                                                                            \
        LAS char* sl = sm + ((c) & 3) * 32768;                                                      \
        _Pragma("unroll") for (int kk = 0; kk < 2; ++kk) {                                          \
            s16x8 bf0 = *(LAS s16x8*)(sl + (kk ? boff1 : boff0));                                   \
            s16x8 bf1 = *(LAS s16x8*)(sl + (kk ? boff1 : boff0) + 2048);                            \
            _Pragma("unroll") for (int ft = 0; ft < 4; ++ft) {                                      \
                s16x8 af = cat8(trread(sl + aoff[ft] + kk * 8192), trread(sl + aoff[ft] + kk * 8192 + 1024)); \
                acc[ft][0] = MFMA(af, bf0, acc[ft][0]);                                             \
                acc[ft][1] = MFMA(af, bf1, acc[ft][1]);                                             \
            }                                                                                       \
        }                                                                                           \
    } while (0)
        WAIT_V(12); BAR(); COMPUTE(0);
        WAIT_V(8); WAIT_L0(); BAR(); STAGE(4); COMPUTE(1);
        WAIT_V(8); WAIT_L0(); BAR(); STAGE(5); COMPUTE(2);
        WAIT_V(8); WAIT_L0(); BAR(); STAGE(6); COMPUTE(3);
        WAIT_V(8); WAIT_L0(); BAR(); STAGE(7); COMPUTE(4);
        kv_prep(sm, w, lane, idx, x, uT);
        WAIT_V(8); BAR(); COMPUTE(5);
        WAIT_V(4); BAR(); COMPUTE(6);
        WAIT_V(0); BAR(); COMPUTE(7);
        char* hF = ws + WS_HF;
#pragma unroll
        for (int pp = 0; pp < 2; ++pp)
#pragma unroll
            for (int st = 0; st < 2; ++st) {
                const int kap = 4 * fp + 2 * fh + pp;
                const int s16 = rp * 8 + 2 * sw + st;
                f32x4 a0 = acc[2 * pp][st], a1 = acc[2 * pp + 1][st];
                v4u o = {pk2(silu(a0[0]), silu(a0[1])), pk2(silu(a0[2]), silu(a0[3])), pk2(silu(a1[0]), silu(a1[1])), pk2(silu(a1[2]), silu(a1[3]))};
                *(GAS v4u*)(hF + (size_t)((((s16 >> 2) * 56 + kap) * 4 + (s16 & 3)) * 1024) + lane * 16) = o;
            }
    } else {
        const int a = (idx - 28) * 8 + x;
#pragma unroll
        for (int i = 0; i < 7; ++i) {
            const int T = 7 * a + i, kap = T >> 2, nq = T & 3;
            glds16(w2 + (32 * kap + 4 * w) * D + 128 * nq, (unsigned)((lane >> 4) * D + (lane & 15) * 8) * 2u, sm + i * 8192 + w * 1024);
        }
        v4u qv[8];
#pragma unroll
        for (int i = 0; i < 8; ++i) qv[i] = *(const GAS v4u*)(q + (size_t)(64 * a + 8 * w + i) * D + lane * 8);
        v4u gv = *(const GAS v4u*)(gw + lane * 8);
        GAS v4u* zp = (GAS v4u*)(ws + WS_ZT) + (size_t)a * 4096 + tid;
        const v4u sent = {SENT, SENT, SENT, SENT};
#pragma unroll
        for (int i = 0; i < 8; ++i) zp[i * 512] = sent;
        float* g = (float*)(ws + WS_G);
        float gsum = 0.f;
#pragma unroll
        for (int i = 0; i < 8; ++i) {
            float s = 0.f;
#pragma unroll
            for (int e = 0; e < 4; ++e) {
                unsigned r = pk2(bflo(qv[i][e]) * bflo(gv[e]), bfhi(qv[i][e]) * bfhi(gv[e]));
                s += bflo(r) + bfhi(r);
            }
#pragma unroll
            for (int m = 32; m >= 1; m >>= 1) s += __shfl_xor(s, m);
            if (lane == i) gsum = s;
        }
        if (lane < 8) {
            float t = rbf(gsum);
            g[64 * a + 8 * w + lane] = rbf(__builtin_amdgcn_rcpf(1.0f + __builtin_amdgcn_exp2f(-1.44269504088896f * t)));
        }
        WAIT_V(0); BAR();
        kv_prep(sm, w, lane, idx, x, uT);
        const int qd = lane >> 4, qq = (lane & 15) >> 2, p = lane & 3;
        char* w2F = ws + WS_W2F;
#pragma unroll
        for (int i = 0; i < 7; ++i) {
            const int T = 7 * a + i, kap = T >> 2, nq = T & 3;
            LAS char* src = sm + i * 8192 + (4 * qd + qq) * 256 + 32 * w + 8 * p;
            s16x8 fr = cat8(trread(src), trread(src + 16 * 256));
            const int N = 8 * nq + w;
            *(GAS s16x8*)(w2F + (size_t)((((N >> 2) * 56 + kap) * 4 + (N & 3)) * 1024) + lane * 16) = fr;
        }
    }
}

constexpr int U_CP = 4416, U_CH = 4 * U_CP, H_CP = 6400, H_CH = 2 * H_CP;
constexpr int L_U = 0, L_H = 2 * U_CH, L_Z = L_H + 2 * H_CH, L_P = L_Z + 8192, P_PITCH = 272, P_W = 64 * P_PITCH;
constexpr int K2_LDS = L_P + 4 * P_W;

struct FillRegs { v2u ua; v4u ub; v4u ha; v2u hb; };
__device__ __forceinline__ FillRegs fill_load(const char* ws, const u16* hk, int d0, int tid) {
    const int ch = tid >> 8, L = tid & 255;
    const char* ur = ws + WS_UT + (size_t)(d0 + ch) * 4096;
    const char* hr = (const char*)hk + (size_t)(d0 + ch) * 4096;
    FillRegs r;
    r.ua = *(const GAS v2u*)(ur + (L == 0 ? 0 : 16 * L - 8));
    r.ub = *(const GAS v4u*)(ur + 16 * L);
    r.ha = *(const GAS v4u*)(hr + 16 * L);
    r.hb = *(const GAS v2u*)(hr + (L == 255 ? 0 : 16 * L + 16));
    return r;
}
__device__ __forceinline__ unsigned ab16(unsigned hi, unsigned lo) { return __builtin_amdgcn_alignbit(hi, lo, 16); }
__device__ __forceinline__ void fill_store(LAS char* sm, FillRegs r, int tid) {
    const int ch = tid >> 8, L = tid & 255;
    if (L == 0) { r.ua.x = 0; r.ua.y = 0; }
    if (L == 255) { r.hb.x = 0; r.hb.y = 0; }
    const unsigned w0 = r.ua.x, w1 = r.ua.y, w2 = r.ub.x, w3 = r.ub.y, w4 = r.ub.z, w5 = r.ub.w;
    const unsigned t0 = ab16(w1, w0), t1 = ab16(w2, w1), t2 = ab16(w3, w2), t3 = ab16(w4, w3), t4 = ab16(w5, w4);
    LAS char* ub = sm + L_U + ch * U_CH;
    LAS char* up = ub + 16 * L + 64;
    *(LAS v4u*)(up) = (v4u){t1, t2, t3, t4};
    *(LAS v4u*)(up + U_CP) = (v4u){w1, w2, w3, w4};
    *(LAS v4u*)(up + 2 * U_CP) = (v4u){t0, t1, t2, t3};
    *(LAS v4u*)(up + 3 * U_CP) = (v4u){w0, w1, w2, w3};
    LAS char* hb = sm + L_H + ch * H_CH;
    LAS char* hp = hb + ((L >> 3) + 8) * 160 + (L & 7) * 16;
    *(LAS v4u*)(hp) = r.ha;
    *(LAS v4u*)(hp + H_CP) = (v4u){r.ha.z, r.ha.w, r.hb.x, r.hb.y};
    const v4u z = {0, 0, 0, 0};
    if (L < 128) {
        const int e = L >> 6, c = L & 63;
        if (L != 127) *(LAS v4u*)(hb + e * H_CP + (c >> 3) * 160 + (c & 7) * 16) = z;
    } else if (L < 160) {
        const int c = (L - 128) >> 3, j = L & 7;
        if (j != 4) *(LAS v4u*)(ub + c * U_CP + (j < 4 ? 16 * j : 16 * (252 + j) + 64)) = z;
    }
    if (L == 0) *(LAS v4u*)(hb + H_CP + 7 * 160 + 7 * 16) = (v4u){0, 0, r.ha.x, r.ha.y};
    if (L == 255) {
        LAS char* e = ub + 16 * 256 + 64;
        *(LAS v4u*)(e) = (v4u){w5 >> 16, 0, 0, 0};
        *(LAS v4u*)(e + U_CP) = (v4u){w5, 0, 0, 0};
        *(LAS v4u*)(e + 2 * U_CP) = (v4u){t4, w5 >> 16, 0, 0};
        *(LAS v4u*)(e + 3 * U_CP) = (v4u){w4, w5, 0, 0};
    }
}

__global__ __launch_bounds__(512, 2) void k2(const u16* __restrict__ q, const u16* __restrict__ hk, const u16* __restrict__ Dv,
                                             char* __restrict__ ws, u16* __restrict__ out) {
    __shared__ __attribute__((aligned(16))) char smem[K2_LDS];
    LAS char* sm = (LAS char*)smem;
    const int b = blockIdx.x, x = b & 7, idx = b >> 3;
    const int tid = threadIdx.x, lane = tid & 63;
    const int w = __builtin_amdgcn_readfirstlane(tid >> 6);
    const int rt = 8 * (x >> 1) + (idx & 7), ct = 4 * (x & 1) + (idx >> 3);
    const int d0 = 64 * x + 2 * idx;
    const int es = 16 * (w & 3) + (lane & 15), ed = 32 * (w >> 2) + 8 * (lane >> 4);
    v4u qv; float gg;

    if (w < 4) {
        FillRegs fr = fill_load(ws, hk, d0, tid);
        const GAS v4u* hp = (const GAS v4u*)(ws + WS_HF) + (size_t)(rt * 56 + 14 * w) * 256 + lane;
        const GAS v4u* wp = (const GAS v4u*)(ws + WS_W2F) + (size_t)(ct * 56 + 14 * w) * 256 + lane;
        constexpr int DEP = 5;
        v4u A[DEP][4], B[DEP][4];
#pragma unroll
        for (int k = 0; k < DEP; ++k) {
            SCHED();
#pragma unroll
            for (int i = 0; i < 4; ++i) { A[k][i] = wp[k * 256 + i * 64]; B[k][i] = hp[k * 256 + i * 64]; }
        }
        SCHED();
        asm volatile("" : "+v"(fr.ua), "+v"(fr.ub), "+v"(fr.ha), "+v"(fr.hb));
        fill_store(sm, fr, tid);
        WAIT_L0(); BAR();
        SCHED();
        f32x4 acc[4][4] = {};
#pragma unroll
        for (int k = 0; k < 14; ++k) {
            s16x8 a[4], bb[4];
#pragma unroll
            for (int i = 0; i < 4; ++i) { a[i] = __builtin_bit_cast(s16x8, A[k % DEP][i]); bb[i] = __builtin_bit_cast(s16x8, B[k % DEP][i]); }
#pragma unroll
            for (int n = 0; n < 4; ++n)
#pragma unroll
                for (int s = 0; s < 4; ++s) acc[n][s] = MFMA(a[n], bb[s], acc[n][s]);
            SCHED();
            if (k + DEP < 14) {
#pragma unroll
                for (int i = 0; i < 4; ++i) { A[k % DEP][i] = wp[(k + DEP) * 256 + i * 64]; B[k % DEP][i] = hp[(k + DEP) * 256 + i * 64]; }
            }
            if (k == 10) {
                qv = *(const GAS v4u*)(q + (size_t)(64 * rt + es) * D + 64 * ct + ed);
                gg = ((const GAS float*)(ws + WS_G))[64 * rt + es];
            }
            SCHED();
        }
        LAS char* pp = sm + L_P + w * P_W + (lane & 15) * P_PITCH + (lane >> 4) * 16;
#pragma unroll
        for (int n = 0; n < 4; ++n)
#pragma unroll
            for (int s = 0; s < 4; ++s) *(LAS f32x4*)(pp + s * 16 * P_PITCH + n * 64) = acc[n][s];
        WAIT_L0(); BAR();
    } else {
        FillRegs fr = fill_load(ws, hk, d0, tid);
        const int cw = w - 4, ch = cw >> 1, half = cw & 1;
        const float dd = bflo(Dv[d0 + ch]);
        qv = *(const GAS v4u*)(q + (size_t)(64 * rt + es) * D + 64 * ct + ed);
        gg = ((const GAS float*)(ws + WS_G))[64 * rt + es];
        fill_store(sm, fr, tid);
        WAIT_L0(); BAR();
        const int m = lane & 15, qd = lane >> 4;
        const int ntA = 3 - half, ntB = half;
        LAS char* ua = sm + L_U + ch * U_CH + (m & 3) * U_CP + 64 + 4096 + 16 * (qd - (m >> 2));
        LAS char* hb = sm + L_H + ch * H_CH + (m >> 3) * H_CP + (m & 7) * 160 + qd * 16;
        f32x4 aA0 = {}, aA1 = {}, aB0 = {}, aB1 = {};
        int kap = 16 * ntA + 16;
        LAS char* up = ua - 64 * kap;
        LAS char* hA = hb;
        s16x8 Wc = *(LAS s16x8*)up;
        const int n1 = 8 * (ntA - ntB);
        for (int it = 0; it < n1; ++it) {
            s16x8 W1 = *(LAS s16x8*)(up + 64);
            s16x8 W2 = *(LAS s16x8*)(up + 128);
            s16x8 B1 = *(LAS s16x8*)(hA + 64);
            s16x8 B2 = *(LAS s16x8*)(hA + 160);
            aA1 = MFMA(Wc, B1, aA1); aA0 = MFMA(W1, B1, aA0);
            aA1 = MFMA(W1, B2, aA1); aA0 = MFMA(W2, B2, aA0);
            Wc = W2; up += 128; hA += 160;
        }
        LAS char* hB = hb;
        const int n2 = 8 * ntB + 8;
        for (int it = 0; it < n2; ++it) {
            s16x8 W1 = *(LAS s16x8*)(up + 64);
            s16x8 W2 = *(LAS s16x8*)(up + 128);
            s16x8 B1 = *(LAS s16x8*)(hA + 64);
            s16x8 B2 = *(LAS s16x8*)(hA + 160);
            s16x8 C1 = *(LAS s16x8*)(hB + 64);
            s16x8 C2 = *(LAS s16x8*)(hB + 160);
            aA1 = MFMA(Wc, B1, aA1); aA0 = MFMA(W1, B1, aA0);
            aB1 = MFMA(Wc, C1, aB1); aB0 = MFMA(W1, C1, aB0);
            aA1 = MFMA(W1, B2, aA1); aA0 = MFMA(W2, B2, aA0);
            aB1 = MFMA(W1, C2, aB1); aB0 = MFMA(W2, C2, aB0);
            Wc = W2; up += 128; hA += 160; hB += 160;
        }
        {
            s16x8 B1 = *(LAS s16x8*)(hA + 64);
            s16x8 C1 = *(LAS s16x8*)(hB + 64);
            aA1 = MFMA(Wc, B1, aA1);
            aB1 = MFMA(Wc, C1, aB1);
        }
        const __amdgpu_buffer_rsrc_t zr = __builtin_amdgcn_make_buffer_rsrc(ws + WS_ZT, 0, 2 << 20, 0x00020000);
        const int b0 = m >> 3, b1 = m & 7;
        LAS char* u3 = sm + L_U + ch * U_CH + 3 * U_CP + 64;
#pragma unroll
        for (int t = 0; t < 2; ++t) {
            const int nt = t ? ntB : ntA;
            v2u zz[2];
#pragma unroll
            for (int mt = 0; mt < 2; ++mt) {
                f32x4 y = t ? (mt ? aB1 : aB0) : (mt ? aA1 : aA0);
                const int s = 8 * qd + 32 * mt + 4 * b0 + 64 * b1 + 512 * nt;
                v2u uu = *(LAS v2u*)(u3 + 2 * (2048 - s));
                unsigned y01 = pk2(y[0], y[1]), y23 = pk2(y[2], y[3]);
                unsigned p01 = pk2(bfhi(uu.y) * dd, bflo(uu.y) * dd), p23 = pk2(bfhi(uu.x) * dd, bflo(uu.x) * dd);
                unsigned z01 = pk2(bflo(y01) + bflo(p01), bfhi(y01) + bfhi(p01));
                unsigned z23 = pk2(bflo(y23) + bflo(p23), bfhi(y23) + bfhi(p23));
                zz[mt].x = z01 == SENT ? 0x7FC07FC0u : z01;
                zz[mt].y = z23 == SENT ? 0x7FC07FC0u : z23;
            }
            v2u snd = b0 ? zz[0] : zz[1], keep = b0 ? zz[1] : zz[0], rcv;
            rcv.x = __builtin_amdgcn_update_dpp(0, (int)snd.x, 0x128, 0xF, 0xF, false);
            rcv.y = __builtin_amdgcn_update_dpp(0, (int)snd.y, 0x128, 0xF, 0xF, false);
            v4u o = b0 ? (v4u){rcv.x, rcv.y, keep.x, keep.y} : (v4u){keep.x, keep.y, rcv.x, rcv.y};
            const int s0 = 8 * qd + 32 * b0 + 64 * b1 + 512 * nt;
            __builtin_amdgcn_raw_buffer_store_b128(o, zr, (d0 + ch) * 4096 + 2 * s0, 0, 16);
        }
        const int zoff = (64 * ct + 16 * cw + (lane >> 3)) * 4096 + rt * 128 + (lane & 7) * 16;
        v4u z0, z1;
        for (unsigned spins = 0; spins < (1u << 16); ++spins) {
            asm volatile("" ::: "memory");
            z0 = __builtin_amdgcn_raw_buffer_load_b128(zr, zoff, 0, 16);
            z1 = __builtin_amdgcn_raw_buffer_load_b128(zr, zoff + 8 * 4096, 0, 16);
            bool ok = z0.x != SENT && z0.y != SENT && z0.z != SENT && z0.w != SENT && z1.x != SENT && z1.y != SENT && z1.z != SENT && z1.w != SENT;
            if (__all(ok)) break;
            __builtin_amdgcn_s_sleep(2);
        }
        *(LAS v4u*)(sm + L_Z + cw * 2048 + lane * 16) = z0;
        *(LAS v4u*)(sm + L_Z + cw * 2048 + 1024 + lane * 16) = z1;
        WAIT_L0(); BAR();
    }
    {
        const int qq = (lane & 15) >> 2, p = lane & 3;
        LAS char* zp = sm + L_Z + (ed + qq) * 128 + 32 * (w & 3) + 8 * p;
        v2u za = __builtin_bit_cast(v2u, trread(zp)), zb = __builtin_bit_cast(v2u, trread(zp + 512));
        LAS char* pp = sm + L_P + es * P_PITCH + ed * 4;
        f32x4 s0 = *(LAS f32x4*)pp, s1 = *(LAS f32x4*)(pp + 16);
#pragma unroll
        for (int i = 1; i < 4; ++i) { s0 += *(LAS f32x4*)(pp + i * P_W); s1 += *(LAS f32x4*)(pp + i * P_W + 16); }
        const float g1 = rbf(1.0f - gg);
        unsigned zw[4] = {za.x, za.y, zb.x, zb.y};
        float bs[8] = {s0[0], s0[1], s0[2], s0[3], s1[0], s1[1], s1[2], s1[3]};
        v4u o;
#pragma unroll
        for (int e = 0; e < 4; ++e) {
            unsigned bb = pk2(bs[2 * e], bs[2 * e + 1]);
            unsigned aa = pk2(bflo(qv[e]) * bflo(zw[e]), bfhi(qv[e]) * bfhi(zw[e]));
            unsigned ga = pk2(gg * bflo(aa), gg * bfhi(aa));
            unsigned gb = pk2(g1 * bflo(bb), g1 * bfhi(bb));
            o[e] = pk2(bflo(ga) + bflo(gb), bfhi(ga) + bfhi(gb));
        }
        *(GAS v4u*)(out + (size_t)(64 * rt + es) * D + 64 * ct + ed) = o;
    }
}

extern "C" void kernel_launch(void* const* d_in, const int* in_sizes, int n_in, void* d_out, int out_size, void* d_ws, size_t ws_size,
                              hipStream_t stream) {
    const u16* q = (const u16*)d_in[0]; const u16* k = (const u16*)d_in[1]; const u16* v = (const u16*)d_in[2];
    const u16* sk = (const u16*)d_in[3]; const u16* hk = (const u16*)d_in[4]; const u16* Dv = (const u16*)d_in[5];
    const u16* w1 = (const u16*)d_in[7]; const u16* w2 = (const u16*)d_in[8]; const u16* gw = (const u16*)d_in[9];
    k1<<<256, 512, 0, stream>>>(q, k, v, sk, w1, w2, gw, (char*)d_ws);
    k2<<<256, 512, 0, stream>>>(q, hk, Dv, (char*)d_ws, (u16*)d_out);
}
```

```cpp
#include <hip/hip_runtime.h>
#include <hip/hip_bf16.h>
#include <stdint.h>
#define MAP84 1

#define LAS __attribute__((address_space(3)))
#define GAS __attribute__((address_space(1)))
typedef unsigned short u16;
typedef short s16x4 __attribute__((ext_vector_type(4)));
typedef short s16x8 __attribute__((ext_vector_type(8)));
typedef float f32x4 __attribute__((ext_vector_type(4)));
typedef unsigned v4u __attribute__((ext_vector_type(4)));
typedef unsigned v2u __attribute__((ext_vector_type(2)));
typedef __bf16 bf16x2_t __attribute__((ext_vector_type(2)));
typedef float f32x2 __attribute__((ext_vector_type(2)));
typedef __amdgpu_buffer_rsrc_t rsrc_t;

constexpr int S = 2048, D = 512, F = 1792;

constexpr int WS_HF = 0;
constexpr int WS_UT = 8 << 20;
constexpr int WS_Z = 10 << 20;
constexpr int WS_G = 12 << 20;
constexpr int WS_FU = WS_G + 65536;
constexpr int WS_FH = WS_FU + 32768;
constexpr int WS_FZ = WS_FH + 32768;
constexpr int WS_E = WS_FZ + 32768;
constexpr int WS_BYTES = 16 << 20;
constexpr int SC1 = 16;
constexpr unsigned SPIN_CAP = 1u << 16;

__device__ __forceinline__ float bflo(unsigned w) { return __uint_as_float(w << 16); }
__device__ __forceinline__ float bfhi(unsigned w) { return __uint_as_float(w & 0xFFFF0000u); }
__device__ __forceinline__ unsigned pk2v(f32x2 f) { return __builtin_bit_cast(unsigned, __builtin_convertvector(f, bf16x2_t)); }
__device__ __forceinline__ unsigned pk2(float lo, float hi) { return pk2v((f32x2){lo, hi}); }
__device__ __forceinline__ float rbf(float f) { return bflo(pk2(f, f)); }
__device__ __forceinline__ s16x4 trread(LAS char* p) { return __builtin_amdgcn_ds_read_tr16_b64_v4i16((LAS s16x4*)p); }
__device__ __forceinline__ s16x8 cat8(s16x4 a, s16x4 b) { return __builtin_shufflevector(a, b, 0, 1, 2, 3, 4, 5, 6, 7); }
__device__ __forceinline__ void glds16(const void* base, unsigned voff, LAS char* l) {
    unsigned keep;
    asm volatile("s_mov_b32 %0, m0\n\ts_mov_b32 m0, %3\n\ts_nop 0\n\tglobal_load_lds_dwordx4 %1, %2\n\ts_mov_b32 m0, %0"
                 : "=&s"(keep) : "v"(voff), "s"(base), "s"((unsigned)(size_t)l) : "memory");
}
#define WAIT_V(n) asm volatile("s_waitcnt vmcnt(" #n ")" ::: "memory")
#define WAIT_V0C() __builtin_amdgcn_s_waitcnt(0x0F70)
#define WAIT_L0() __builtin_amdgcn_s_waitcnt(0xC07F)
#define BAR() asm volatile("s_barrier" ::: "memory")
#define SCHED() __builtin_amdgcn_sched_barrier(0)
#define CBAR() asm volatile("" ::: "memory")
#define ST_DECL
#define ST(i)
#define ST_END(k)
#define MFMA(a, b, c) __builtin_amdgcn_mfma_f32_16x16x32_bf16(a, b, c, 0, 0, 0)

__device__ __forceinline__ v4u ld128(rsrc_t rs, int off) { return __builtin_amdgcn_raw_buffer_load_b128(rs, off, 0, SC1); }
__device__ __forceinline__ v2u ld64(rsrc_t rs, int off) { return __builtin_amdgcn_raw_buffer_load_b64(rs, off, 0, SC1); }
__device__ __forceinline__ unsigned ld32(rsrc_t rs, int off) { return __builtin_amdgcn_raw_buffer_load_b32(rs, off, 0, SC1); }
__device__ __forceinline__ void st128(v4u v, rsrc_t rs, int off) { __builtin_amdgcn_raw_buffer_store_b128(v, rs, off, 0, SC1); }
__device__ __forceinline__ void st64(v2u v, rsrc_t rs, int off) { __builtin_amdgcn_raw_buffer_store_b64(v, rs, off, 0, SC1); }
__device__ __forceinline__ void st32(unsigned v, rsrc_t rs, int off) { __builtin_amdgcn_raw_buffer_store_b32(v, rs, off, 0, SC1); }
__device__ __forceinline__ void poll_flags(rsrc_t rs, int off, unsigned tag) {
#pragma unroll 1
    for (unsigned spins = 0; spins < SPIN_CAP; ++spins) {
        CBAR();
        unsigned v = ld32(rs, off);
        if (__all(v == tag)) break;
        __builtin_amdgcn_s_sleep(4);
    }
    CBAR();
}
__device__ __forceinline__ v4u silu8(f32x4 a0, f32x4 a1) {
    f32x2 x[4] = {{a0[0], a0[1]}, {a0[2], a0[3]}, {a1[0], a1[1]}, {a1[2], a1[3]}};
    f32x2 t[4];
#pragma unroll
    for (int i = 0; i < 4; ++i) t[i] = x[i] * -1.44269504088896f;
#pragma unroll
    for (int i = 0; i < 4; ++i) { t[i].x = __builtin_amdgcn_exp2f(t[i].x); t[i].y = __builtin_amdgcn_exp2f(t[i].y); }
#pragma unroll
    for (int i = 0; i < 4; ++i) t[i] = t[i] + 1.0f;
#pragma unroll
    for (int i = 0; i < 4; ++i) { t[i].x = __builtin_amdgcn_rcpf(t[i].x); t[i].y = __builtin_amdgcn_rcpf(t[i].y); }
    v4u o;
#pragma unroll
    for (int i = 0; i < 4; ++i) o[i] = pk2v(x[i] * t[i]);
    return o;
}

constexpr int L_CV = 98304;
constexpr int L_T = L_CV + 24576;
constexpr int U_CP = 4416, U_CH = 4 * U_CP, H_CP = 6400, H_CH = 2 * H_CP;
constexpr int L_U = L_CV, L_H = L_CV + 2 * U_CH;
constexpr int LDS_BYTES = 163840;
constexpr int L_ZS = L_H + 2 * H_CH;
constexpr int L_FL = LDS_BYTES - 128;
enum { F_W2OK = 0, F_C7 = 4, F_FILL = 8, F_CONV = 12, F_C01 = 16, F_PART = 20, F_TAG = 24 };

__device__ __forceinline__ void lds_set(LAS char* sm, int i, int lane) {
    if (lane == 0) *(volatile LAS unsigned*)(sm + L_FL + 4 * i) = 1u;
}
__device__ __forceinline__ void lds_wait(LAS char* sm, int i0, int mask, int lane) {
#pragma unroll 1
    for (unsigned spins = 0; spins < (1u << 22); ++spins) {
        unsigned v = *(volatile LAS unsigned*)(sm + L_FL + 4 * (i0 + (lane & mask)));
        if (__all(v != 0u)) break;
        __builtin_amdgcn_s_sleep(1);
    }
    CBAR();
}

__device__ __forceinline__ v4u kv_prep(LAS char* kvw, int e, int lane, bool zero_halo) {
    const int gq = lane >> 4, qq = (lane & 15) >> 2, p = lane & 3;
    LAS char* kb = kvw + (8 * e + 4 + qq) * 128 + 32 * gq + 8 * p;
    v2u k0 = __builtin_bit_cast(v2u, trread(kb));
    v2u k1 = __builtin_bit_cast(v2u, trread(kb + 512));
    v2u k2 = __builtin_bit_cast(v2u, trread(kb + 1024));
    LAS char* vb = kvw + 3072 + (8 * e + qq) * 128 + 32 * gq + 8 * p;
    v2u v0 = __builtin_bit_cast(v2u, trread(vb));
    v2u v1 = __builtin_bit_cast(v2u, trread(vb + 512));
    v2u sk = *(LAS v2u*)(kvw + 5120 + 8 * lane);
    if (zero_halo) { k0.x = 0; k0.y = 0; }
    float kk[12] = {bflo(k0.x), bfhi(k0.x), bflo(k0.y), bfhi(k0.y), bflo(k1.x), bfhi(k1.x), bflo(k1.y), bfhi(k1.y),
                    bflo(k2.x), bfhi(k2.x), bflo(k2.y), bfhi(k2.y)};
    float vv[8] = {bflo(v0.x), bfhi(v0.x), bflo(v0.y), bfhi(v0.y), bflo(v1.x), bfhi(v1.x), bflo(v1.y), bfhi(v1.y)};
    const float w0 = bflo(sk.x), w1 = bfhi(sk.x), w2 = bflo(sk.y), w3 = bfhi(sk.y);
    float ks[8];
#pragma unroll
    for (int i = 0; i < 8; ++i) ks[i] = kk[i + 1] * w3 + kk[i + 2] * w2 + kk[i + 3] * w1 + kk[i + 4] * w0;
    float u[8];
#pragma unroll
    for (int i = 0; i < 8; i += 2) {
        unsigned r = pk2(ks[i], ks[i + 1]);
        u[i] = bflo(r) * vv[i];
        u[i + 1] = bfhi(r) * vv[i + 1];
    }
    return (v4u){pk2(u[7], u[6]), pk2(u[5], u[4]), pk2(u[3], u[2]), pk2(u[1], u[0])};
}

__device__ __forceinline__ unsigned ab16(unsigned hi, unsigned lo) { return __builtin_amdgcn_alignbit(hi, lo, 16); }
__device__ __forceinline__ void fill_u(LAS char* sm, v2u ua, v4u ubv, int ch, int L) {
    if (L == 0) { ua.x = 0; ua.y = 0; }
    const unsigned w0 = ua.x, w1 = ua.y, w2 = ubv.x, w3 = ubv.y, w4 = ubv.z, w5 = ubv.w;
    const unsigned t0 = ab16(w1, w0), t1 = ab16(w2, w1), t2 = ab16(w3, w2), t3 = ab16(w4, w3), t4 = ab16(w5, w4);
    LAS char* ub = sm + L_U + ch * U_CH;
    LAS char* up = ub + 16 * L + 64;
    *(LAS v4u*)(up) = (v4u){t1, t2, t3, t4};
    *(LAS v4u*)(up + U_CP) = (v4u){w1, w2, w3, w4};
    *(LAS v4u*)(up + 2 * U_CP) = (v4u){t0, t1, t2, t3};
    *(LAS v4u*)(up + 3 * U_CP) = (v4u){w0, w1, w2, w3};
    if (L == 255) {
        LAS char* e = ub + 16 * 256 + 64;
        *(LAS v4u*)(e) = (v4u){w5 >> 16, 0, 0, 0};
        *(LAS v4u*)(e + U_CP) = (v4u){w5, 0, 0, 0};
        *(LAS v4u*)(e + 2 * U_CP) = (v4u){t4, w5 >> 16, 0, 0};
        *(LAS v4u*)(e + 3 * U_CP) = (v4u){w4, w5, 0, 0};
    }
}
__device__ __forceinline__ void fill_h(LAS char* sm, v4u ha, v2u hbv, int ch, int L) {
    if (L == 255) { hbv.x = 0; hbv.y = 0; }
    LAS char* hb = sm + L_H + ch * H_CH;
    LAS char* hp = hb + ((L >> 3) + 8) * 160 + (L & 7) * 16;
    *(LAS v4u*)(hp) = ha;
    *(LAS v4u*)(hp + H_CP) = (v4u){ha.z, ha.w, hbv.x, hbv.y};
    if (L == 0) *(LAS v4u*)(hb + H_CP + 7 * 160 + 7 * 16) = (v4u){0, 0, ha.x, ha.y};
}
__device__ __forceinline__ void fill_pads_h(LAS char* sm, int ch, int half, int lane) {
    const v4u z = {0, 0, 0, 0};
    LAS char* hb = sm + L_H + ch * H_CH;
    if (!(half == 1 && lane == 63)) *(LAS v4u*)(hb + half * H_CP + (lane >> 3) * 160 + (lane & 7) * 16) = z;
}
__device__ __forceinline__ void fill_pads_u(LAS char* sm, int ch, int half, int lane) {
    const v4u z = {0, 0, 0, 0};
    if (lane < 16) {
        LAS char* ub = sm + L_U + ch * U_CH;
        const int c = 2 * half + (lane >> 3), jx = lane & 7;
        if (jx != 4) *(LAS v4u*)(ub + c * U_CP + (jx < 4 ? 16 * jx : 16 * (252 + jx) + 64)) = z;
    }
}

template <int NTA, int NTB, class Hook>
__device__ __forceinline__ void conv_body(LAS char* ua, LAS char* hb, f32x4& aA0, f32x4& aA1, f32x4& aB0, f32x4& aB1, Hook hook) {
    constexpr int NA = 16 * NTA + 17, NB = 16 * NTB + 17, T0 = NA - NB, PF = 4;
    constexpr int HK = NTA == 3 ? 36 : 26, HS = NTA == 3 ? 3 : 2;
    LAS char* up = ua - 64 * (NA - 1);
    asm volatile("" : "+v"(up), "+v"(hb));
    s16x8 W[NA + 1], BA[NA], BB[NB];
#define HOFF(t) (((t) & 1) ? (((t) + 1) / 2) * 160 : ((t) / 2) * 160 + 64)
    W[0] = *(LAS s16x8*)up;
#pragma unroll
    for (int t = -PF; t < NA; ++t) {
        const int tl = t + PF;
        if (tl < NA) {
            if (tl + 1 < NA) W[tl + 1] = *(LAS s16x8*)(up + 64 * (tl + 1));
            BA[tl] = *(LAS s16x8*)(hb + HOFF(tl));
            if (tl >= T0) BB[tl - T0] = *(LAS s16x8*)(hb + HOFF(tl - T0));
        }
        SCHED();
        if (t >= 0) {
            aA1 = MFMA(W[t], BA[t], aA1);
            if (t + 1 < NA) aA0 = MFMA(W[t + 1], BA[t], aA0);
            if (t >= T0) {
                aB1 = MFMA(W[t], BB[t - T0], aB1);
                if (t + 1 < NA) aB0 = MFMA(W[t + 1], BB[t - T0], aB0);
            }
            SCHED();
            if (t >= HK && (t - HK) % HS == 0 && (t - HK) / HS <= 8) { hook((t - HK) / HS); SCHED(); }
        }
    }
}

__global__ __launch_bounds__(512, 2) void fused(const u16* __restrict__ q, const u16* __restrict__ kin, const u16* __restrict__ vin,
                                                const u16* __restrict__ sk, const u16* __restrict__ hk, const u16* __restrict__ Dv,
                                                const u16* __restrict__ w1, const u16* __restrict__ w2, const u16* __restrict__ gw,
                                                char* __restrict__ ws, u16* __restrict__ out) {
    __shared__ __attribute__((aligned(16))) char smem[LDS_BYTES];
    LAS char* sm = (LAS char*)smem;
    const int b = blockIdx.x, x = b & 7, idx = b >> 3;
    const int tid = threadIdx.x, lane = tid & 63;
    const int w = __builtin_amdgcn_readfirstlane(tid >> 6);
    const int j = w & 3;
    const rsrc_t rs = __builtin_amdgcn_make_buffer_rsrc(ws, 0, WS_BYTES, 0x00020000);
    ST_DECL; ST(0);
    const int rt = 8 * (x >> 1) + (idx & 7), ct = 4 * (x & 1) + (idx >> 3);
    const int d0 = 64 * x + 2 * idx;
    const int c16 = lane & 15, qd = lane >> 4, qq = c16 >> 2, p4 = lane & 3;
    const bool isG = idx < 28;
    const int rp = 4 * (x >> 1) + (idx & 3), fp = 7 * (x & 1) + (idx >> 2);
    const int ch = j >> 1, half = j & 1;

#define PLACE(i) (((i) >> 1) * 32768 + j * 8192 + ((i) & 1) * 4096)
    const int w2rd = (4 * qd + qq) * 128 + 32 * ((2 * qd + (qq >> 1)) & 3) + 8 * p4;
#define W2READ(dst, addr)                                                                    \
    do {                                                                                     \
        _Pragma("unroll") for (int n_ = 0; n_ < 4; ++n_) {                                   \
            LAS char* a_ = sm + (addr) + (w2rd ^ (n_ << 5));                                 \
            dst[n_] = cat8(trread(a_), trread(a_ + 2048));                                   \
        }                                                                                    \
    } while (0)
    const unsigned w2off = (unsigned)((lane >> 3) * D + ((((((lane & 7) >> 1) ^ qd) << 1) | (lane & 1)) * 8)) * 2u;
    const u16* w2C = w2 + (size_t)(224 * j) * D + 64 * ct;
#define W2STAGE(base, k_, addr)                                                              \
    do {                                                                                     \
        _Pragma("unroll") for (int i_ = 0; i_ < 4; ++i_)                                     \
            glds16(base + (32 * (k_) + 8 * i_) * D, w2off, sm + (addr) + i_ * 1024);         \
    } while (0)
    const int kpl = ((7 * w) >> 1) + (lane & 3);
    const int fh_off = WS_FH + ((rt >> 1) * 16 + (kpl >> 1)) * 128 + ((rt & 1) + 2 * (kpl & 1)) * 4;
    const int h_off = WS_HF + (rt * 56 + 7 * w) * 4096 + lane * 16;
    const int es = 16 * j + c16, ed = 32 * (w >> 2) + 8 * qd;
    unsigned tag;
    v4u qv;
#define HLOAD(k_) do { _Pragma("unroll") for (int i_ = 0; i_ < 4; ++i_) hbuf[k_][i_] = ld128(rs, h_off + (k_) * 4096 + i_ * 1024); SCHED(); } while (0)
#define G2MMA(wf, k_) do { _Pragma("unroll") for (int n_ = 0; n_ < 4; ++n_) _Pragma("unroll") for (int s_ = 0; s_ < 4; ++s_) \
        acc2[n_][s_] = MFMA(wf[n_], __builtin_bit_cast(s16x8, hbuf[k_][s_]), acc2[n_][s_]); SCHED(); } while (0)
#define LPIECE(i) ((i) == 15 ? PLACE(4) + 2048 : L_CV + j * 16384 + (i) * 1024)

    if (w < 4) {
        unsigned ev, dvr;
        asm volatile("global_load_dword %0, %2, %3 sc1\n\tglobal_load_ushort %1, %2, %4" : "=&v"(ev), "=&v"(dvr) : "v"(0), "s"(ws + WS_E), "s"(Dv + d0 + ch) : "memory");
        LAS char* kvw = sm + L_CV + j * 6144;
        {
            int r = 64 * idx + 16 * j - 8 + (lane >> 3);
            const unsigned co = (unsigned)(64 * x + (lane & 7) * 8) * 2u;
            glds16(kin, (unsigned)((r < 0 ? 0 : r) * D) * 2u + co, kvw);
            const unsigned o1 = (unsigned)((r + 8) * D) * 2u + co;
            glds16(kin, o1, kvw + 1024);
            glds16(kin + 8 * D, o1, kvw + 2048);
            glds16(vin, o1, kvw + 3072);
            glds16(vin + 8 * D, o1, kvw + 4096);
            glds16(sk, (unsigned)(256 * x + (lane & 31) * 8) * 2u, kvw + 5120);
        }
        unsigned dvs;
        asm volatile("s_waitcnt vmcnt(0)\n\tv_readfirstlane_b32 %0, %2\n\tv_readfirstlane_b32 %1, %3\n\ts_nop 3" : "=s"(tag), "=s"(dvs) : "v"(ev), "v"(dvr) : "memory");
        tag += 1u;
        if (w == 0 && lane < 32) *(LAS unsigned*)(sm + L_FL + 4 * lane) = lane == F_TAG ? tag : 0u;
        ST(1);
        {
            v4u o0 = kv_prep(kvw, 0, lane, idx == 0 && j == 0);
            v4u o1 = kv_prep(kvw, 1, lane, false);
            *(LAS v4u*)(sm + L_T + lane * 144 + (7 - 2 * j) * 16) = o0;
            *(LAS v4u*)(sm + L_T + lane * 144 + (6 - 2 * j) * 16) = o1;
        }
        WAIT_L0(); SCHED();
        ST(2);
        BAR();
#define UT_STORE() do { _Pragma("unroll") for (int e_ = 0; e_ < 2; ++e_) { const int c_ = 16 * j + 8 * e_ + (lane >> 3); \
            v4u o_ = *(LAS v4u*)(sm + L_T + c_ * 144 + (lane & 7) * 16); \
            st128(o_, rs, WS_UT + (64 * x + c_) * 4096 + (1984 - 64 * idx) * 2 + (lane & 7) * 16); } } while (0)
        const int fu_st = WS_FU + (x * 32 + idx) * 128;
        if (isG) {
            const int sh = j & 1, fh = j >> 1;
            int boff0 = (64 * sh + c16) * 128 + 16 * (qd ^ (c16 >> 1));
            int boff1 = (64 * sh + c16) * 128 + 16 * ((4 + qd) ^ (c16 >> 1));
            const int key = qq | ((qd & 1) << 2);
            int aoff[4];
#pragma unroll
            for (int ft = 0; ft < 4; ++ft) aoff[ft] = 16384 + (8 * qd + qq) * 256 + 32 * ((4 * fh + ft) ^ key) + 8 * p4;
            f32x4 acc[4][4] = {};
            s16x8 fa[2][2][4], fb[2][2][4];
#define READ(c)                                                                                     \
    do {                                                                                            \
        LAS char* sl = sm + ((c) % 3) * 32768;                                                      \
        _Pragma("unroll") for (int kk = 0; kk < 2; ++kk) {                                          \
            _Pragma("unroll") for (int ft = 0; ft < 4; ++ft) {                                      \
                fb[(c) & 1][kk][ft] = *(LAS s16x8*)(sl + (kk ? boff1 : boff0) + 2048 * ft);         \
                fa[(c) & 1][kk][ft] = cat8(trread(sl + aoff[ft] + kk * 8192), trread(sl + aoff[ft] + kk * 8192 + 1024)); \
            }                                                                                       \
        }                                                                                           \
    } while (0)
#define MMA(c)                                                                                      \
    do {                                                                                            \
        _Pragma("unroll") for (int kk = 0; kk < 2; ++kk)                                            \
            _Pragma("unroll") for (int ft = 0; ft < 4; ++ft)                                        \
                _Pragma("unroll") for (int st = 0; st < 4; ++st)                                    \
                    acc[ft][st] = MFMA(fa[(c) & 1][kk][ft], fb[(c) & 1][kk][st], acc[ft][st]);      \
    } while (0)
#define ILV() do { _Pragma("unroll") for (int g_ = 0; g_ < 24; ++g_) { __builtin_amdgcn_sched_group_barrier(0x100, 1, 0); __builtin_amdgcn_sched_group_barrier(0x8, 1, 0); } \
                   __builtin_amdgcn_sched_group_barrier(0x8, 8, 0); SCHED(); } while (0)
            SCHED(); UT_STORE(); READ(0); SCHED(); WAIT_L0();
            BAR(); SCHED(); READ(1); MMA(0); ILV(); WAIT_L0();
            BAR(); SCHED(); READ(2); MMA(1); ILV(); WAIT_L0();
            WAIT_V(0);
            BAR();
            if (tid == 0) st32(tag, rs, fu_st);
            SCHED(); READ(3); MMA(2); ILV(); WAIT_L0();
            ST(3);
            BAR(); SCHED(); READ(4); MMA(3); ILV(); WAIT_L0();
            BAR(); SCHED(); READ(5); MMA(4); ILV(); WAIT_L0();
            BAR(); SCHED(); READ(6); MMA(5); ILV(); WAIT_L0();
            BAR(); SCHED();
            ST(4);
            READ(7); MMA(6); ILV(); WAIT_L0();
            lds_set(sm, F_C7 + j, lane);
            SCHED();
            MMA(7);
            ST(5);
            const int hst = WS_HF + ((2 * rp + sh) * 56 + 4 * fp + 2 * fh) * 4096 + lane * 16;
#pragma unroll
            for (int pp = 0; pp < 2; ++pp)
#pragma unroll
                for (int st = 0; st < 4; ++st) st128(silu8(acc[2 * pp][st], acc[2 * pp + 1][st]), rs, hst + pp * 4096 + st * 1024);
            ST(6);
        } else {
            UT_STORE();
            const int r0 = 512 * (x >> 1) + 64 * (4 * (x & 1) + (idx - 28)) + 16 * j;
            v4u gv = *(const GAS v4u*)(gw + lane * 8);
            float gsum = 0.f;
#pragma unroll
            for (int hh = 0; hh < 2; ++hh) {
                v4u qr[8];
#pragma unroll
                for (int i = 0; i < 8; ++i) qr[i] = *(const GAS v4u*)(q + (size_t)(r0 + 8 * hh + i) * D + lane * 8);
                if (hh == 0) {
                    WAIT_V(0);
                    BAR();
                    if (tid == 0) st32(tag, rs, fu_st);
                }
#pragma unroll
                for (int i = 0; i < 8; ++i) {
                    float s = 0.f;
#pragma unroll
                    for (int e = 0; e < 4; ++e) {
                        unsigned r = pk2(bflo(qr[i][e]) * bflo(gv[e]), bfhi(qr[i][e]) * bfhi(gv[e]));
                        s += bflo(r) + bfhi(r);
                    }
#pragma unroll
                    for (int m = 32; m >= 1; m >>= 1) s += __shfl_xor(s, m);
                    if (lane == 8 * hh + i) gsum = s;
                }
            }
            if (lane < 16) {
                float t = rbf(gsum);
                float g = rbf(__builtin_amdgcn_rcpf(1.0f + __builtin_amdgcn_exp2f(-1.44269504088896f * t)));
                st64((v2u){__float_as_uint(g), tag}, rs, WS_G + (r0 + lane) * 8);
            }
        }
        lds_wait(sm, F_FILL + 2 * ch, 1, lane);
        ST(7);
        s16x8 wl[7][4];
        f32x4 aA0 = {}, aA1 = {}, aB0 = {}, aB1 = {};
        {
            LAS char* ua_ = sm + L_U + ch * U_CH + (c16 & 3) * U_CP + 64 + 4096 + 16 * (qd - (c16 >> 2));
            LAS char* hb_ = sm + L_H + ch * H_CH + (c16 >> 3) * H_CP + (c16 & 7) * 160 + qd * 16;
            auto hook = [&](int i) {
                if (i == 0) {
                    WAIT_V(0);
                    if (isG && lane == 0) st32(tag, rs, WS_FH + (rp * 16 + fp) * 128 + j * 4);
                    ST(8);
                    lds_wait(sm, F_C01 + j, 0, lane);
                    W2READ(wl[0], PLACE(0)); W2READ(wl[1], PLACE(1));
                    WAIT_L0();
                } else {
                    glds16(w2C + (32 * 5 + 8 * (i - 1)) * D, w2off, sm + PLACE(0) + (i - 1) * 1024);
                }
            };
            if (half == 0) conv_body<3, 0>(ua_, hb_, aA0, aA1, aB0, aB1, hook);
            else conv_body<2, 1>(ua_, hb_, aA0, aA1, aB0, aB1, hook);
        }
        ST(9);
        {
            const float dd = bflo(dvs);
            const int b0 = c16 >> 3, b1 = c16 & 7;
            LAS char* u3 = sm + L_U + ch * U_CH + 3 * U_CP + 64;
            LAS char* zs = sm + L_ZS + j * 1024;
#pragma unroll
            for (int t = 0; t < 2; ++t) {
                const int nt = t ? half : 3 - half;
#pragma unroll
                for (int mt = 0; mt < 2; ++mt) {
                    f32x4 y = t ? (mt ? aB1 : aB0) : (mt ? aA1 : aA0);
                    const int sl = 8 * qd + 32 * mt + 4 * b0 + 64 * b1;
                    v2u uu = *(LAS v2u*)(u3 + 2 * (2048 - 512 * nt - sl));
                    unsigned y01 = pk2(y[0], y[1]), y23 = pk2(y[2], y[3]);
                    unsigned p01 = pk2(bfhi(uu.y) * dd, bflo(uu.y) * dd), p23 = pk2(bfhi(uu.x) * dd, bflo(uu.x) * dd);
                    v2u zz;
                    zz.x = pk2(bflo(y01) + bflo(p01), bfhi(y01) + bfhi(p01));
                    zz.y = pk2(bflo(y23) + bflo(p23), bfhi(y23) + bfhi(p23));
                    *(LAS v2u*)(zs + 2 * sl) = zz;
                }
                CBAR();
                v4u zo = *(LAS v4u*)(zs + lane * 16);
                CBAR();
                st128(zo, rs, WS_Z + (d0 + ch) * 4096 + nt * 1024 + lane * 16);
            }
            WAIT_L0(); SCHED();
            lds_set(sm, F_CONV + j, lane);
        }
        ST(10);
        poll_flags(rs, fh_off, tag);
        WAIT_V(0);
        if (lane == 0) st32(tag, rs, WS_FZ + (x * 32 + idx) * 128 + j * 4);
        lds_wait(sm, F_W2OK + j, 0, lane);
        ST(11);
        v4u hbuf[7][4];
        f32x4 acc2[4][4] = {};
        HLOAD(0); HLOAD(1); HLOAD(2); HLOAD(3); HLOAD(4); HLOAD(5); HLOAD(6);
        qv = *(const GAS v4u*)(q + (size_t)(64 * rt + es) * D + 64 * ct + ed);
        SCHED();
        G2MMA(wl[0], 0);
        W2READ(wl[2], PLACE(2)); SCHED(); G2MMA(wl[1], 1);
        W2READ(wl[3], PLACE(3)); SCHED(); G2MMA(wl[2], 2);
        W2READ(wl[4], PLACE(5)); SCHED(); G2MMA(wl[3], 3);
        W2READ(wl[5], PLACE(0)); SCHED(); G2MMA(wl[4], 4);
        W2READ(wl[6], PLACE(1)); SCHED(); G2MMA(wl[5], 5);
        lds_wait(sm, F_PART + j, 0, lane);
#pragma unroll
        for (int n = 0; n < 4; ++n)
#pragma unroll
            for (int s = 0; s < 4; ++s) acc2[n][s] += *(LAS f32x4*)(sm + LPIECE(n * 4 + s) + lane * 16);
        SCHED();
        G2MMA(wl[6], 6);
        ST(12);
#pragma unroll
        for (int s = 0; s < 4; ++s) {
            LAS char* pp = sm + PLACE(s) + c16 * 256;
#pragma unroll
            for (int n = 0; n < 4; ++n) *(LAS f32x4*)(pp + (((4 * n + qd) ^ c16) << 4)) = acc2[n][s];
        }
    } else {
        const int fu_off = WS_FU + (x * 32 + (lane & 31)) * 128;
        const u16* w2L = w2 + (size_t)(224 * w) * D + 64 * ct;
        const char* hrow = (const char*)hk + (size_t)(d0 + ch) * 4096;
        const int urow = WS_UT + (d0 + ch) * 4096;
        const int L0 = 128 * half + lane, L1 = L0 + 64;
        v4u ub[2]; v2u ua[2];
        bool have_u = false;
        v4u ha0, ha1; v2u hb0, hb1;
        asm volatile("global_load_dwordx4 %0, %4, %6\n\tglobal_load_dwordx2 %1, %4, %6 offset:16\n\t"
                     "global_load_dwordx4 %2, %4, %6 offset:1024\n\tglobal_load_dwordx2 %3, %5, %6"
                     : "=&v"(ha0), "=&v"(hb0), "=&v"(ha1), "=&v"(hb1)
                     : "v"(16 * L0), "v"(L1 == 255 ? 0 : 16 * L1 + 16), "s"(hrow) : "memory");
        const unsigned xs0 = (unsigned)((rp * 128 + 32 * j + (lane >> 3)) * D + (((lane & 7) ^ qd) * 8)) * 2u;
        const unsigned xs1 = (unsigned)((rp * 128 + 32 * j + 8 + (lane >> 3)) * D + (((lane & 7) ^ (4 + qd)) * 8)) * 2u;
        const unsigned ws0 = (unsigned)((16 * j + qd) * F + fp * 128 + (((((c16 >> 1) ^ qd) << 1) | (lane & 1)) * 8)) * 2u;
        const unsigned ws1 = (unsigned)((16 * j + 8 + qd) * F + fp * 128 + (((((c16 >> 1) ^ (qd | 4)) << 1) | (lane & 1)) * 8)) * 2u;
#define STAGE(c)                                                          \
    do {                                                                  \
        LAS char* sl = sm + ((c) % 3) * 32768 + 4096 * j;                 \
        glds16(q + (c) * 64, xs0, sl);                                    \
        glds16(w1 + (c) * 64 * F, ws0, sl + 16384);                       \
        glds16(q + (c) * 64, xs1, sl + 1024);                             \
        glds16(w1 + (c) * 64 * F + 4 * F, ws0, sl + 16384 + 1024);        \
        glds16(q + (c) * 64 + 16 * D, xs0, sl + 2048);                    \
        glds16(w1 + (c) * 64 * F, ws1, sl + 16384 + 2048);                \
        glds16(q + (c) * 64 + 16 * D, xs1, sl + 3072);                    \
        glds16(w1 + (c) * 64 * F + 4 * F, ws1, sl + 16384 + 3072);        \
    } while (0)
        if (isG) { STAGE(0); STAGE(1); }
        else { W2STAGE(w2L, 4, PLACE(4)); W2STAGE(w2L, 5, PLACE(5)); W2STAGE(w2L, 0, PLACE(0)); W2STAGE(w2L, 1, PLACE(1)); }
        ST(1);
        asm volatile("s_waitcnt vmcnt(16)" : "+v"(ha0), "+v"(hb0), "+v"(ha1), "+v"(hb1) :: "memory");
        fill_pads_h(sm, ch, half, lane);
        fill_h(sm, ha0, hb0, ch, L0); fill_h(sm, ha1, hb1, ch, L1);
        SCHED();
        if (isG) {
            WAIT_V(8); BAR(); STAGE(2);
            ST(2);
            WAIT_V(8); BAR(); STAGE(3);
            WAIT_V(8); BAR(); STAGE(4);
            WAIT_V(8); BAR(); STAGE(5);
            ST(3);
            WAIT_V(8); BAR(); STAGE(6);
            WAIT_V(8); BAR(); STAGE(7);
            tag = __builtin_amdgcn_readfirstlane(*(volatile LAS unsigned*)(sm + L_FL + 4 * F_TAG));
            WAIT_V(8); BAR();
            unsigned pr;
            asm volatile("global_load_dword %0, %1, %2 sc1" : "=v"(pr) : "v"(fu_off), "s"(ws) : "memory");
            W2STAGE(w2L, 4, PLACE(4)); W2STAGE(w2L, 5, PLACE(5));
            WAIT_V(9); BAR();
            ST(4);
            asm volatile("s_waitcnt vmcnt(8)" : "+v"(pr) :: "memory");
            have_u = __all(pr == tag);
            if (have_u) {
                asm volatile("global_load_dwordx2 %0, %4, %6 sc1\n\tglobal_load_dwordx4 %1, %5, %6 sc1\n\t"
                             "global_load_dwordx2 %2, %5, %6 offset:1016 sc1\n\tglobal_load_dwordx4 %3, %5, %6 offset:1024 sc1"
                             : "=&v"(ua[0]), "=&v"(ub[0]), "=&v"(ua[1]), "=&v"(ub[1])
                             : "v"(urow + (L0 == 0 ? 0 : 16 * L0 - 8)), "v"(urow + 16 * L0), "s"(ws) : "memory");
                W2STAGE(w2L, 0, PLACE(0)); W2STAGE(w2L, 1, PLACE(1));
                asm volatile("s_waitcnt vmcnt(8)" : "+v"(ua[0]), "+v"(ub[0]), "+v"(ua[1]), "+v"(ub[1]) :: "memory");
            } else {
                W2STAGE(w2L, 0, PLACE(0)); W2STAGE(w2L, 1, PLACE(1));
            }
        } else {
            W2STAGE(w2L, 2, PLACE(2)); W2STAGE(w2L, 3, PLACE(3));
            BAR(); BAR();
            tag = __builtin_amdgcn_readfirstlane(*(volatile LAS unsigned*)(sm + L_FL + 4 * F_TAG));
            __builtin_amdgcn_s_sleep(32);
        }
        if (!have_u) {
            poll_flags(rs, fu_off, tag);
            ua[0] = ld64(rs, urow + (L0 == 0 ? 0 : 16 * L0 - 8));
            ub[0] = ld128(rs, urow + 16 * L0);
            ua[1] = ld64(rs, urow + 16 * L1 - 8);
            ub[1] = ld128(rs, urow + 16 * L1);
            WAIT_V0C();
        }
        ST(5);
        fill_pads_u(sm, ch, half, lane);
        fill_u(sm, ua[0], ub[0], ch, L0); fill_u(sm, ua[1], ub[1], ch, L1);
        WAIT_L0(); SCHED();
        lds_set(sm, F_FILL + j, lane);
        if (isG) {
            lds_wait(sm, F_C7, 3, lane);
            W2STAGE(w2L, 2, PLACE(2)); W2STAGE(w2L, 3, PLACE(3));
        }
        ST(6);
        s16x8 wr[6][4];
        WAIT_V(8);
        W2READ(wr[0], PLACE(0)); W2READ(wr[1], PLACE(1)); W2READ(wr[4], PLACE(4)); W2READ(wr[5], PLACE(5));
        WAIT_L0(); SCHED();
        W2STAGE(w2C, 0, PLACE(0)); W2STAGE(w2C, 1, PLACE(1));
        WAIT_V(8);
        W2READ(wr[2], PLACE(2)); W2READ(wr[3], PLACE(3));
        WAIT_L0(); SCHED();
        W2STAGE(w2L, 6, PLACE(4)); W2STAGE(w2C, 2, PLACE(2));
        WAIT_V(8);
        lds_set(sm, F_C01 + j, lane);
        ST(7);
        W2STAGE(w2C, 3, PLACE(3)); W2STAGE(w2C, 4, PLACE(5));
        if (b == 255 && w == 4) {
#pragma unroll 1
            for (int i = 0; i < 4; ++i) poll_flags(rs, WS_FU + (i * 64 + lane) * 128, tag);
            if (lane == 0) st32(tag, rs, WS_E);
        }
        ST(10);
        poll_flags(rs, fh_off, tag);
        ST(11);
        v4u hbuf[7][4];
        f32x4 acc2[4][4] = {};
        HLOAD(0); HLOAD(1); HLOAD(2); HLOAD(3);
        lds_set(sm, F_W2OK + j, lane);
        SCHED();
        G2MMA(wr[0], 0); HLOAD(4); HLOAD(5);
        G2MMA(wr[1], 1); HLOAD(6);
        qv = *(const GAS v4u*)(q + (size_t)(64 * rt + es) * D + 64 * ct + ed);
        SCHED();
        s16x8 wl[4];
        G2MMA(wr[2], 2);
        G2MMA(wr[3], 3);
        G2MMA(wr[4], 4);
        W2READ(wl, PLACE(4)); SCHED(); G2MMA(wr[5], 5);
        G2MMA(wl, 6);
        ST(12);
        const int nt = rt >> 3, hz = (nt == 3 || nt == 0) ? 0 : 1;
        const int fz_off = WS_FZ + (ct * 32 + 8 * j + (c16 >> 1)) * 128 + ((c16 & 1) * 2 + hz) * 4;
        unsigned zf = ld32(rs, fz_off);
        v2u gr = ld64(rs, WS_G + (64 * rt + lane) * 8);
        SCHED();
        lds_wait(sm, F_CONV, 3, lane);
#pragma unroll
        for (int n = 0; n < 4; ++n)
#pragma unroll
            for (int s = 0; s < 4; ++s) *(LAS f32x4*)(sm + LPIECE(n * 4 + s) + lane * 16) = acc2[n][s];
        WAIT_L0(); SCHED();
        lds_set(sm, F_PART + j, lane);
#pragma unroll 1
        for (unsigned spins = 0; spins < SPIN_CAP; ++spins) {
            if (__all(zf == tag && gr.y == tag)) break;
            __builtin_amdgcn_s_sleep(2);
            CBAR();
            zf = ld32(rs, fz_off);
            gr = ld64(rs, WS_G + (64 * rt + lane) * 8);
        }
        CBAR();
        const int zoff = WS_Z + (64 * ct + 16 * j + (lane >> 3)) * 4096 + rt * 128 + (lane & 7) * 16;
        v4u z0 = ld128(rs, zoff), z1 = ld128(rs, zoff + 8 * 4096);
        LAS char* zi = sm + PLACE(4) + lane * 16;
        *(LAS v4u*)(zi) = z0;
        *(LAS v4u*)(zi + 1024) = z1;
        if (j == 0) *(LAS unsigned*)(sm + 65536 + 3072 + lane * 4) = gr.x;
    }
    WAIT_L0();
    ST(13);
    BAR();
    {
        const int zr = ed + qq;
        LAS char* zp = sm + 65536 + (zr >> 4) * 8192 + (zr & 15) * 128 + 32 * j + 8 * p4;
        v2u za = __builtin_bit_cast(v2u, trread(zp)), zb = __builtin_bit_cast(v2u, trread(zp + 512));
        const float gg = *(LAS float*)(sm + 65536 + 3072 + es * 4);
        LAS char* pp = sm + (j >> 1) * 32768 + (j & 1) * 4096 + c16 * 256;
        const int sl0 = ((ed >> 2) ^ c16) << 4, sl1 = (((ed >> 2) + 1) ^ c16) << 4;
        f32x4 s0 = *(LAS f32x4*)(pp + sl0), s1 = *(LAS f32x4*)(pp + sl1);
#pragma unroll
        for (int i = 1; i < 4; ++i) { s0 += *(LAS f32x4*)(pp + i * 8192 + sl0); s1 += *(LAS f32x4*)(pp + i * 8192 + sl1); }
        const float g1 = rbf(1.0f - gg);
        unsigned zw[4] = {za.x, za.y, zb.x, zb.y};
        float bs[8] = {s0[0], s0[1], s0[2], s0[3], s1[0], s1[1], s1[2], s1[3]};
        v4u o;
#pragma unroll
        for (int e = 0; e < 4; ++e) {
            unsigned bb = pk2(bs[2 * e], bs[2 * e + 1]);
            unsigned aa = pk2(bflo(qv[e]) * bflo(zw[e]), bfhi(qv[e]) * bfhi(zw[e]));
            unsigned ga = pk2(gg * bflo(aa), gg * bfhi(aa));
            unsigned gb = pk2(g1 * bflo(bb), g1 * bfhi(bb));
            o[e] = pk2(bflo(ga) + bflo(gb), bfhi(ga) + bfhi(gb));
        }
        *(GAS v4u*)(out + (size_t)(64 * rt + es) * D + 64 * ct + ed) = o;
    }
    ST(14);
    ST(15);
    ST_END(0);
}

extern "C" void kernel_launch(void* const* d_in, const int* in_sizes, int n_in, void* d_out, int out_size, void* d_ws, size_t ws_size,
                              hipStream_t stream) {
    const u16* q = (const u16*)d_in[0]; const u16* k = (const u16*)d_in[1]; const u16* v = (const u16*)d_in[2];
    const u16* sk = (const u16*)d_in[3]; const u16* hk = (const u16*)d_in[4]; const u16* Dv = (const u16*)d_in[5];
    const u16* w1 = (const u16*)d_in[7]; const u16* w2 = (const u16*)d_in[8]; const u16* gw = (const u16*)d_in[9];
    fused<<<256, 512, 0, stream>>>(q, k, v, sk, hk, Dv, w1, w2, gw, (char*)d_ws, (u16*)d_out);
}
```

```cpp
#include <hip/hip_runtime.h>
#include <hip/hip_bf16.h>
#include <stdint.h>

#define LAS __attribute__((address_space(3)))
#define GAS __attribute__((address_space(1)))
typedef unsigned short u16;
typedef short s16x4 __attribute__((ext_vector_type(4)));
typedef short s16x8 __attribute__((ext_vector_type(8)));
typedef float f32x4 __attribute__((ext_vector_type(4)));
typedef unsigned v4u __attribute__((ext_vector_type(4)));
typedef unsigned v2u __attribute__((ext_vector_type(2)));
typedef __bf16 bf16x2_t __attribute__((ext_vector_type(2)));
typedef float f32x2 __attribute__((ext_vector_type(2)));
typedef __amdgpu_buffer_rsrc_t rsrc_t;

constexpr int S = 2048, D = 512, F = 1792;
#define HKV 14

constexpr int WS_HF = 0;
constexpr int WS_UT = 8 << 20;
constexpr int WS_Z = 10 << 20;
constexpr int WS_G = 14 << 20;
constexpr int WS_FU = WS_G + 65536;
constexpr int WS_FH = WS_FU + 32768;
constexpr int WS_E = WS_FH + 32768;
constexpr int WS_BYTES = 16 << 20;
constexpr int SC1 = 16;
constexpr unsigned SPIN_CAP = 1u << 16;

__device__ __forceinline__ float bflo(unsigned w) { return __uint_as_float(w << 16); }
__device__ __forceinline__ float bfhi(unsigned w) { return __uint_as_float(w & 0xFFFF0000u); }
__device__ __forceinline__ unsigned pk2v(f32x2 f) { return __builtin_bit_cast(unsigned, __builtin_convertvector(f, bf16x2_t)); }
__device__ __forceinline__ unsigned pk2(float lo, float hi) { return pk2v((f32x2){lo, hi}); }
__device__ __forceinline__ float rbf(float f) { return bflo(pk2(f, f)); }
__device__ __forceinline__ s16x4 trread(LAS char* p) { return __builtin_amdgcn_ds_read_tr16_b64_v4i16((LAS s16x4*)p); }
__device__ __forceinline__ s16x8 cat8(s16x4 a, s16x4 b) { return __builtin_shufflevector(a, b, 0, 1, 2, 3, 4, 5, 6, 7); }
__device__ __forceinline__ void glds16(const void* base, unsigned voff, LAS char* l) {
    unsigned keep;
    asm volatile("s_mov_b32 %0, m0\n\ts_mov_b32 m0, %3\n\ts_nop 0\n\tglobal_load_lds_dwordx4 %1, %2\n\ts_mov_b32 m0, %0"
                 : "=&s"(keep) : "v"(voff), "s"(base), "s"((unsigned)(size_t)l) : "memory");
}
#define WAIT_V(n) asm volatile("s_waitcnt vmcnt(" #n ")" ::: "memory")
#define WAIT_V0C() __builtin_amdgcn_s_waitcnt(0x0F70)
#define WAIT_L0() __builtin_amdgcn_s_waitcnt(0xC07F)
#define BAR() asm volatile("s_barrier" ::: "memory")
#define SCHED() __builtin_amdgcn_sched_barrier(0)
#define CBAR() asm volatile("" ::: "memory")
#define ST_DECL
#define ST(i)
#define ST_END(k)
#define MFMA(a, b, c) __builtin_amdgcn_mfma_f32_16x16x32_bf16(a, b, c, 0, 0, 0)

__device__ __forceinline__ v4u ld128(rsrc_t rs, int off) { return __builtin_amdgcn_raw_buffer_load_b128(rs, off, 0, SC1); }
__device__ __forceinline__ v2u ld64(rsrc_t rs, int off) { return __builtin_amdgcn_raw_buffer_load_b64(rs, off, 0, SC1); }
__device__ __forceinline__ unsigned ld32(rsrc_t rs, int off) { return __builtin_amdgcn_raw_buffer_load_b32(rs, off, 0, SC1); }
__device__ __forceinline__ void st128(v4u v, rsrc_t rs, int off) { __builtin_amdgcn_raw_buffer_store_b128(v, rs, off, 0, SC1); }
__device__ __forceinline__ void st64(v2u v, rsrc_t rs, int off) { __builtin_amdgcn_raw_buffer_store_b64(v, rs, off, 0, SC1); }
__device__ __forceinline__ void st32(unsigned v, rsrc_t rs, int off) { __builtin_amdgcn_raw_buffer_store_b32(v, rs, off, 0, SC1); }
__device__ __forceinline__ void poll_flags(rsrc_t rs, int off, unsigned tag) {
#pragma unroll 1
    for (unsigned spins = 0; spins < SPIN_CAP; ++spins) {
        CBAR();
        unsigned v = ld32(rs, off);
        if (__all(v == tag)) break;
        __builtin_amdgcn_s_sleep(4);
    }
    CBAR();
}
__device__ __forceinline__ v4u silu8(f32x4 a0, f32x4 a1) {
    f32x2 x[4] = {{a0[0], a0[1]}, {a0[2], a0[3]}, {a1[0], a1[1]}, {a1[2], a1[3]}};
    f32x2 t[4];
#pragma unroll
    for (int i = 0; i < 4; ++i) t[i] = x[i] * -1.44269504088896f;
#pragma unroll
    for (int i = 0; i < 4; ++i) { t[i].x = __builtin_amdgcn_exp2f(t[i].x); t[i].y = __builtin_amdgcn_exp2f(t[i].y); }
#pragma unroll
    for (int i = 0; i < 4; ++i) t[i] = t[i] + 1.0f;
#pragma unroll
    for (int i = 0; i < 4; ++i) { t[i].x = __builtin_amdgcn_rcpf(t[i].x); t[i].y = __builtin_amdgcn_rcpf(t[i].y); }
    v4u o;
#pragma unroll
    for (int i = 0; i < 4; ++i) o[i] = pk2v(x[i] * t[i]);
    return o;
}

constexpr int L_CV = 98304;
constexpr int L_T = L_CV + 24576;
constexpr int U_CP = 4416, U_CH = 4 * U_CP, H_CP = 6400, H_CH = 2 * H_CP;
constexpr int L_U = L_CV, L_H = L_CV + 2 * U_CH;
constexpr int LDS_BYTES = 163840;
constexpr int L_ZS = L_H + 2 * H_CH;
constexpr int L_FL = LDS_BYTES - 128;
enum { F_W2OK = 0, F_C7 = 4, F_FILL = 8, F_CONV = 12, F_C01 = 16, F_PART = 20, F_TAG = 24, F_UT = 25 };

__device__ __forceinline__ void lds_set(LAS char* sm, int i, int lane) {
    if (lane == 0) *(volatile LAS unsigned*)(sm + L_FL + 4 * i) = 1u;
}
__device__ __forceinline__ void lds_wait(LAS char* sm, int i0, int mask, int lane) {
#pragma unroll 1
    for (unsigned spins = 0; spins < (1u << 22); ++spins) {
        unsigned v = *(volatile LAS unsigned*)(sm + L_FL + 4 * (i0 + (lane & mask)));
        if (__all(v != 0u)) break;
        __builtin_amdgcn_s_sleep(1);
    }
    CBAR();
}

__device__ __forceinline__ v4u kv_prep(LAS char* kvw, int e, int lane, bool zero_halo) {
    const int gq = lane >> 4, qq = (lane & 15) >> 2, p = lane & 3;
    LAS char* kb = kvw + (8 * e + 4 + qq) * 128 + 32 * gq + 8 * p;
    v2u k0 = __builtin_bit_cast(v2u, trread(kb));
    v2u k1 = __builtin_bit_cast(v2u, trread(kb + 512));
    v2u k2 = __builtin_bit_cast(v2u, trread(kb + 1024));
    LAS char* vb = kvw + 3072 + (8 * e + qq) * 128 + 32 * gq + 8 * p;
    v2u v0 = __builtin_bit_cast(v2u, trread(vb));
    v2u v1 = __builtin_bit_cast(v2u, trread(vb + 512));
    v2u sk = *(LAS v2u*)(kvw + 5120 + 8 * lane);
    if (zero_halo) { k0.x = 0; k0.y = 0; }
    float kk[12] = {bflo(k0.x), bfhi(k0.x), bflo(k0.y), bfhi(k0.y), bflo(k1.x), bfhi(k1.x), bflo(k1.y), bfhi(k1.y),
                    bflo(k2.x), bfhi(k2.x), bflo(k2.y), bfhi(k2.y)};
    float vv[8] = {bflo(v0.x), bfhi(v0.x), bflo(v0.y), bfhi(v0.y), bflo(v1.x), bfhi(v1.x), bflo(v1.y), bfhi(v1.y)};
    const float w0 = bflo(sk.x), w1 = bfhi(sk.x), w2 = bflo(sk.y), w3 = bfhi(sk.y);
    float ks[8];
#pragma unroll
    for (int i = 0; i < 8; ++i) ks[i] = kk[i + 1] * w3 + kk[i + 2] * w2 + kk[i + 3] * w1 + kk[i + 4] * w0;
    float u[8];
#pragma unroll
    for (int i = 0; i < 8; i += 2) {
        unsigned r = pk2(ks[i], ks[i + 1]);
        u[i] = bflo(r) * vv[i];
        u[i + 1] = bfhi(r) * vv[i + 1];
    }
    return (v4u){pk2(u[7], u[6]), pk2(u[5], u[4]), pk2(u[3], u[2]), pk2(u[1], u[0])};
}

__device__ __forceinline__ unsigned ab16(unsigned hi, unsigned lo) { return __builtin_amdgcn_alignbit(hi, lo, 16); }
__device__ __forceinline__ void fill_u(LAS char* sm, v2u ua, v4u ubv, int ch, int L) {
    if (L == 0) { ua.x = 0; ua.y = 0; }
    const unsigned w0 = ua.x, w1 = ua.y, w2 = ubv.x, w3 = ubv.y, w4 = ubv.z, w5 = ubv.w;
    const unsigned t0 = ab16(w1, w0), t1 = ab16(w2, w1), t2 = ab16(w3, w2), t3 = ab16(w4, w3), t4 = ab16(w5, w4);
    LAS char* ub = sm + L_U + ch * U_CH;
    LAS char* up = ub + 16 * L + 64;
    *(LAS v4u*)(up) = (v4u){t1, t2, t3, t4};
    *(LAS v4u*)(up + U_CP) = (v4u){w1, w2, w3, w4};
    *(LAS v4u*)(up + 2 * U_CP) = (v4u){t0, t1, t2, t3};
    *(LAS v4u*)(up + 3 * U_CP) = (v4u){w0, w1, w2, w3};
    if (L == 255) {
        LAS char* e = ub + 16 * 256 + 64;
        *(LAS v4u*)(e) = (v4u){w5 >> 16, 0, 0, 0};
        *(LAS v4u*)(e + U_CP) = (v4u){w5, 0, 0, 0};
        *(LAS v4u*)(e + 2 * U_CP) = (v4u){t4, w5 >> 16, 0, 0};
        *(LAS v4u*)(e + 3 * U_CP) = (v4u){w4, w5, 0, 0};
    }
}
__device__ __forceinline__ void fill_h(LAS char* sm, v4u ha, v2u hbv, int ch, int L) {
    if (L == 255) { hbv.x = 0; hbv.y = 0; }
    LAS char* hb = sm + L_H + ch * H_CH;
    LAS char* hp = hb + ((L >> 3) + 8) * 160 + (L & 7) * 16;
    *(LAS v4u*)(hp) = ha;
    *(LAS v4u*)(hp + H_CP) = (v4u){ha.z, ha.w, hbv.x, hbv.y};
    if (L == 0) *(LAS v4u*)(hb + H_CP + 7 * 160 + 7 * 16) = (v4u){0, 0, ha.x, ha.y};
}
__device__ __forceinline__ void fill_pads_h(LAS char* sm, int ch, int half, int lane) {
    const v4u z = {0, 0, 0, 0};
    LAS char* hb = sm + L_H + ch * H_CH;
    if (!(half == 1 && lane == 63)) *(LAS v4u*)(hb + half * H_CP + (lane >> 3) * 160 + (lane & 7) * 16) = z;
}
__device__ __forceinline__ void fill_pads_u(LAS char* sm, int ch, int half, int lane) {
    const v4u z = {0, 0, 0, 0};
    if (lane < 16) {
        LAS char* ub = sm + L_U + ch * U_CH;
        const int c = 2 * half + (lane >> 3), jx = lane & 7;
        if (jx != 4) *(LAS v4u*)(ub + c * U_CP + (jx < 4 ? 16 * jx : 16 * (252 + jx) + 64)) = z;
    }
}

template <int NTA, int NTB, class Hook>
__device__ __forceinline__ void conv_body(LAS char* ua, LAS char* hb, f32x4& aA0, f32x4& aA1, f32x4& aB0, f32x4& aB1, Hook hook) {
    constexpr int NA = 16 * NTA + 17, NB = 16 * NTB + 17, T0 = NA - NB, PF = 4;
    constexpr int HK = HKV, HS = NTA == 3 ? 4 : 3;
    LAS char* up = ua - 64 * (NA - 1);
    asm volatile("" : "+v"(up), "+v"(hb));
    s16x8 W[NA + 1], BA[NA], BB[NB];
#define HOFF(t) (((t) & 1) ? (((t) + 1) / 2) * 160 : ((t) / 2) * 160 + 64)
    W[0] = *(LAS s16x8*)up;
#pragma unroll
    for (int t = -PF; t < NA; ++t) {
        const int tl = t + PF;
        if (tl < NA) {
            if (tl + 1 < NA) W[tl + 1] = *(LAS s16x8*)(up + 64 * (tl + 1));
            BA[tl] = *(LAS s16x8*)(hb + HOFF(tl));
            if (tl >= T0) BB[tl - T0] = *(LAS s16x8*)(hb + HOFF(tl - T0));
        }
        SCHED();
        if (t >= 0) {
            aA1 = MFMA(W[t], BA[t], aA1);
            if (t + 1 < NA) aA0 = MFMA(W[t + 1], BA[t], aA0);
            if (t >= T0) {
                aB1 = MFMA(W[t], BB[t - T0], aB1);
                if (t + 1 < NA) aB0 = MFMA(W[t + 1], BB[t - T0], aB0);
            }
            SCHED();
            if (t >= HK && (t - HK) % HS == 0 && (t - HK) / HS <= 8) { hook((t - HK) / HS); SCHED(); }
        }
    }
}

__global__ __launch_bounds__(512, 2) void fused(const u16* __restrict__ q, const u16* __restrict__ kin, const u16* __restrict__ vin,
                                                const u16* __restrict__ sk, const u16* __restrict__ hk, const u16* __restrict__ Dv,
                                                const u16* __restrict__ w1, const u16* __restrict__ w2, const u16* __restrict__ gw,
                                                char* __restrict__ ws, u16* __restrict__ out) {
    __shared__ __attribute__((aligned(16))) char smem[LDS_BYTES];
    LAS char* sm = (LAS char*)smem;
    const int b = blockIdx.x, x = b & 7, idx = b >> 3;
    const int tid = threadIdx.x, lane = tid & 63;
    const int w = __builtin_amdgcn_readfirstlane(tid >> 6);
    const int j = w & 3;
    const rsrc_t rs = __builtin_amdgcn_make_buffer_rsrc(ws, 0, WS_BYTES, 0x00020000);
    ST_DECL; ST(0);
    unsigned long long pc; unsigned len;
    asm volatile(".Lfused_pc:\n\ts_getpc_b64 %0\n\ts_mov_b32 %1, .Lfused_end-.Lfused_pc" : "=s"(pc), "=s"(len));
    const int rt = 8 * (x >> 1) + (idx & 7), ct = 4 * (x & 1) + (idx >> 3);
    const int d0 = 64 * x + 2 * idx;
    const int c16 = lane & 15, qd = lane >> 4, qq = c16 >> 2, p4 = lane & 3;
    const bool isG = idx < 28;
    const int rp = 4 * (x >> 1) + (idx & 3), fp = 7 * (x & 1) + (idx >> 2);
    const int ch = j >> 1, half = j & 1;
    const int L0 = 128 * half + lane, L1 = L0 + 64;

#define PLACE(i) (((i) >> 1) * 32768 + j * 8192 + ((i) & 1) * 4096)
    const int w2rd = (4 * qd + qq) * 128 + 32 * ((2 * qd + (qq >> 1)) & 3) + 8 * p4;
#define W2READ(dst, addr)                                                                    \
    do {                                                                                     \
        _Pragma("unroll") for (int n_ = 0; n_ < 4; ++n_) {                                   \
            LAS char* a_ = sm + (addr) + (w2rd ^ (n_ << 5));                                 \
            dst[n_] = cat8(trread(a_), trread(a_ + 2048));                                   \
        }                                                                                    \
    } while (0)
    const unsigned w2off = (unsigned)((lane >> 3) * D + ((((((lane & 7) >> 1) ^ qd) << 1) | (lane & 1)) * 8)) * 2u;
    const u16* w2C = w2 + (size_t)(224 * j) * D + 64 * ct;
#define W2STAGE(base, k_, addr)                                                              \
    do {                                                                                     \
        _Pragma("unroll") for (int i_ = 0; i_ < 4; ++i_)                                     \
            glds16(base + (32 * (k_) + 8 * i_) * D, w2off, sm + (addr) + i_ * 1024);         \
    } while (0)
    const int h_off = WS_HF + (rt * 56 + 7 * w) * 4096 + lane * 16;
    const int es = 16 * j + c16, ed = 32 * (w >> 2) + 8 * qd;
    unsigned tag;
    v4u qv;
#define HLOAD(k_) do { _Pragma("unroll") for (int i_ = 0; i_ < 4; ++i_) hbuf[k_][i_] = ld128(rs, h_off + (k_) * 4096 + i_ * 1024); SCHED(); } while (0)
#define G2MMA(wf, k_) do { _Pragma("unroll") for (int n_ = 0; n_ < 4; ++n_) _Pragma("unroll") for (int s_ = 0; s_ < 4; ++s_) \
        acc2[n_][s_] = MFMA(wf[n_], __builtin_bit_cast(s16x8, hbuf[k_][s_]), acc2[n_][s_]); SCHED(); } while (0)
#define LPIECE(i) ((i) == 15 ? PLACE(4) + 2048 : L_CV + j * 16384 + (i) * 1024)

    if (w < 4) {
        unsigned ev, dvr, tch, dvs;
        LAS char* kvw = sm + L_CV + j * 6144;
        {
            const int r_ = 64 * idx + 16 * j - 8 + (lane >> 3);
            const unsigned co_ = (unsigned)(64 * x + (lane & 7) * 8) * 2u;
            glds16(kin, (unsigned)((r_ < 0 ? 0 : r_) * D) * 2u + co_, kvw);
            const unsigned o1_ = (unsigned)((r_ + 8) * D) * 2u + co_;
            glds16(kin, o1_, kvw + 1024);
            glds16(vin, o1_, kvw + 3072);
            glds16(sk, (unsigned)(256 * x + (lane & 31) * 8) * 2u, kvw + 5120);
            glds16(kin + 8 * D, o1_, kvw + 2048);
            glds16(vin + 8 * D, o1_, kvw + 4096);
        }
        asm volatile("global_load_dword %0, %2, %3 sc1\n\tglobal_load_ushort %1, %2, %4" : "=&v"(ev), "=&v"(dvr) : "v"(0), "s"(ws + WS_E), "s"(Dv + d0 + ch) : "memory");
        {
            const char* tb; unsigned to;
            const int fpc = fp < 14 ? fp : 13, ixc = isG ? idx : 0;
            if (j == 0) { const int id = 128 + fpc * 64 + lane; tb = (const char*)q; to = (unsigned)((rp * 128 + (id & 127)) * 1024 + (id >> 7) * 128); }
            else if (j == 1) { tb = (const char*)w1; to = (unsigned)((32 * rp + (lane >> 1)) * (F * 2) + fpc * 256 + (lane & 1) * 128); }
            else if (j == 2) { tb = (const char*)w2; to = (unsigned)((16 * ((x >> 1) * 28 + ixc) + (lane >> 2)) * 1024 + (x & 1) * 512 + (lane & 3) * 128); }
            else { tb = (const char*)pc; to = (unsigned)(idx * 16 + c16) * 128u; to = to < len ? to : 0u; }
            asm volatile("global_load_dword %0, %1, %2" : "=v"(tch) : "v"(to), "s"(tb) : "memory");
        }
        if (w == 0 && lane < 32 && lane != F_TAG) *(LAS unsigned*)(sm + L_FL + 4 * lane) = 0u;
        WAIT_V(5);
        ST(1);
        *(LAS v4u*)(sm + L_T + lane * 144 + (7 - 2 * j) * 16) = kv_prep(kvw, 0, lane, idx == 0 && j == 0);
        SCHED(); WAIT_V(3);
        *(LAS v4u*)(sm + L_T + lane * 144 + (6 - 2 * j) * 16) = kv_prep(kvw, 1, lane, false);
        WAIT_L0(); SCHED();
        ST(2);
        BAR();
#define UT_STORE() do { _Pragma("unroll") for (int e_ = 0; e_ < 2; ++e_) { const int c_ = 16 * j + 8 * e_ + (lane >> 3); \
            v4u o_ = *(LAS v4u*)(sm + L_T + c_ * 144 + (lane & 7) * 16); \
            st128(o_, rs, WS_UT + (64 * x + c_) * 4096 + (1984 - 64 * idx) * 2 + (lane & 7) * 16); } } while (0)
#define GET_TAG() do { \
            asm volatile("s_waitcnt vmcnt(0)\n\tv_readfirstlane_b32 %0, %2\n\tv_readfirstlane_b32 %1, %3\n\ts_nop 3" : "=s"(tag), "=s"(dvs) : "v"(ev), "v"(dvr), "v"(tch) : "memory"); \
            tag += 1u; \
            if (tid == 0) *(LAS unsigned*)(sm + L_FL + 4 * F_TAG) = tag; } while (0)
        const int fu_st = WS_FU + (x * 32 + idx) * 128;
        const int urow = WS_UT + (d0 + ch) * 4096;
        v4u ub0, ub1; v2u ua0, ua1;
#define FILL_LOAD() do { \
            lds_wait(sm, F_UT, 0, lane); \
            ua0 = ld64(rs, urow + (L0 == 0 ? 0 : 16 * L0 - 8)); ub0 = ld128(rs, urow + 16 * L0); \
            ua1 = ld64(rs, urow + 16 * L1 - 8); ub1 = ld128(rs, urow + 16 * L1); \
            SCHED(); } while (0)
        if (isG) {
            const int sh = j & 1, fh = j >> 1;
            int boff0 = (64 * sh + c16) * 128 + 16 * (qd ^ (c16 >> 1));
            int boff1 = (64 * sh + c16) * 128 + 16 * ((4 + qd) ^ (c16 >> 1));
            const int key = qq | ((qd & 1) << 2);
            int aoff[4];
#pragma unroll
            for (int ft = 0; ft < 4; ++ft) aoff[ft] = 16384 + (8 * qd + qq) * 256 + 32 * ((4 * fh + ft) ^ key) + 8 * p4;
            f32x4 acc[4][4] = {};
            s16x8 fa[2][2][4], fb[2][2][4];
#define READ(c)                                                                                     \
    do {                                                                                            \
        LAS char* sl = sm + ((c) % 3) * 32768;                                                      \
        _Pragma("unroll") for (int kk = 0; kk < 2; ++kk) {                                          \
            _Pragma("unroll") for (int ft = 0; ft < 4; ++ft) {                                      \
                fb[(c) & 1][kk][ft] = *(LAS s16x8*)(sl + (kk ? boff1 : boff0) + 2048 * ft);         \
                fa[(c) & 1][kk][ft] = cat8(trread(sl + aoff[ft] + kk * 8192), trread(sl + aoff[ft] + kk * 8192 + 1024)); \
            }                                                                                       \
        }                                                                                           \
    } while (0)
#define MMA(c)                                                                                      \
    do {                                                                                            \
        _Pragma("unroll") for (int kk = 0; kk < 2; ++kk)                                            \
            _Pragma("unroll") for (int ft = 0; ft < 4; ++ft)                                        \
                _Pragma("unroll") for (int st = 0; st < 4; ++st)                                    \
                    acc[ft][st] = MFMA(fa[(c) & 1][kk][ft], fb[(c) & 1][kk][st], acc[ft][st]);      \
    } while (0)
#define ILV() do { _Pragma("unroll") for (int g_ = 0; g_ < 24; ++g_) { __builtin_amdgcn_sched_group_barrier(0x100, 1, 0); __builtin_amdgcn_sched_group_barrier(0x8, 1, 0); } \
                   __builtin_amdgcn_sched_group_barrier(0x8, 8, 0); SCHED(); } while (0)
            SCHED(); UT_STORE(); READ(0); SCHED(); WAIT_L0();
            BAR(); SCHED(); READ(1); MMA(0); ILV(); WAIT_L0();
            BAR(); SCHED(); READ(2); MMA(1); ILV();
            GET_TAG(); WAIT_L0();
            BAR();
            if (tid == 0) st32(tag, rs, fu_st);
            SCHED(); READ(3); MMA(2); ILV(); WAIT_L0();
            ST(3);
            BAR(); SCHED(); READ(4); MMA(3); ILV(); WAIT_L0();
            BAR(); SCHED(); READ(5); MMA(4); ILV(); WAIT_L0();
            BAR(); SCHED(); READ(6); MMA(5); ILV(); WAIT_L0();
            BAR(); SCHED();
            ST(4);
            READ(7); MMA(6); ILV(); WAIT_L0();
            lds_set(sm, F_C7 + j, lane);
            FILL_LOAD();
            MMA(7);
            ST(5);
            const int hst = WS_HF + ((2 * rp + sh) * 56 + 4 * fp + 2 * fh) * 4096 + lane * 16;
#pragma unroll
            for (int pp = 0; pp < 2; ++pp)
#pragma unroll
                for (int st = 0; st < 4; ++st) st128(silu8(acc[2 * pp][st], acc[2 * pp + 1][st]), rs, hst + pp * 4096 + st * 1024);
            ST(6);
        } else {
            UT_STORE();
            const int r0 = 512 * (x >> 1) + 64 * (4 * (x & 1) + (idx - 28)) + 16 * j;
            v4u gv = *(const GAS v4u*)(gw + lane * 8);
            float gs[2];
#pragma unroll 1
            for (int hh = 0; hh < 2; ++hh) {
                v4u qr[8];
#pragma unroll
                for (int i = 0; i < 8; ++i) qr[i] = *(const GAS v4u*)(q + (size_t)(r0 + 8 * hh + i) * D + lane * 8);
                if (hh == 0) {
                    GET_TAG(); WAIT_L0();
                    BAR();
                    if (tid == 0) st32(tag, rs, fu_st);
                }
                float s[8];
#pragma unroll
                for (int i = 0; i < 8; ++i) {
                    s[i] = 0.f;
#pragma unroll
                    for (int e = 0; e < 4; ++e) {
                        unsigned r = pk2(bflo(qr[i][e]) * bflo(gv[e]), bfhi(qr[i][e]) * bfhi(gv[e]));
                        s[i] += bflo(r) + bfhi(r);
                    }
                }
#pragma unroll
                for (int i = 0; i < 4; ++i) { const bool hi = lane & 32; float snd = hi ? s[i] : s[i + 4], kp = hi ? s[i + 4] : s[i]; s[i] = kp + __shfl_xor(snd, 32); }
#pragma unroll
                for (int i = 0; i < 2; ++i) { const bool hi = lane & 16; float snd = hi ? s[i] : s[i + 2], kp = hi ? s[i + 2] : s[i]; s[i] = kp + __shfl_xor(snd, 16); }
                { const bool hi = lane & 8; float snd = hi ? s[0] : s[1], kp = hi ? s[1] : s[0]; s[0] = kp + __shfl_xor(snd, 8); }
                s[0] += __shfl_xor(s[0], 4); s[0] += __shfl_xor(s[0], 2); s[0] += __shfl_xor(s[0], 1);
                if (hh == 0) gs[0] = s[0]; else gs[1] = s[0];
            }
            if ((lane & 7) < 2) {
                float t = rbf((lane & 1) ? gs[1] : gs[0]);
                float g = rbf(__builtin_amdgcn_rcpf(1.0f + __builtin_amdgcn_exp2f(-1.44269504088896f * t)));
                st64((v2u){__float_as_uint(g), tag}, rs, WS_G + (r0 + 8 * (lane & 1) + (lane >> 3)) * 8);
            }
            FILL_LOAD();
            WAIT_V0C();
        }
        fill_pads_u(sm, ch, half, lane);
        fill_u(sm, ua0, ub0, ch, L0); fill_u(sm, ua1, ub1, ch, L1);
        WAIT_L0(); SCHED();
        lds_set(sm, F_FILL + j, lane);
        lds_wait(sm, F_FILL + (j ^ 1), 0, lane);
        ST(7);
        s16x8 wl[7][4];
        f32x4 aA0 = {}, aA1 = {}, aB0 = {}, aB1 = {};
        {
            LAS char* ua_ = sm + L_U + ch * U_CH + (c16 & 3) * U_CP + 64 + 4096 + 16 * (qd - (c16 >> 2));
            LAS char* hb_ = sm + L_H + ch * H_CH + (c16 >> 3) * H_CP + (c16 & 7) * 160 + qd * 16;
            auto hook = [&](int i) {
                if (i == 0) {
                    WAIT_V(0);
                    if (isG && lane == 0) st32(tag, rs, WS_FH + (rp * 16 + fp) * 128 + j * 4);
                    ST(8);
                    lds_wait(sm, F_C01 + j, 0, lane);
                    W2READ(wl[0], PLACE(0)); W2READ(wl[1], PLACE(1));
                    WAIT_L0();
                } else {
                    glds16(w2C + (32 * 5 + 8 * (i - 1)) * D, w2off, sm + PLACE(0) + (i - 1) * 1024);
                }
            };
            if (half == 0) conv_body<3, 0>(ua_, hb_, aA0, aA1, aB0, aB1, hook);
            else conv_body<2, 1>(ua_, hb_, aA0, aA1, aB0, aB1, hook);
        }
        ST(9);
        {
            const float dd = bflo(dvs);
            const int b0 = c16 >> 3, b1 = c16 & 7;
            LAS char* u3 = sm + L_U + ch * U_CH + 3 * U_CP + 64;
            LAS char* zs = sm + L_ZS + j * 1024;
            v2u zz[2][2];
#pragma unroll
            for (int t = 0; t < 2; ++t) {
                const int nt = t ? half : 3 - half;
#pragma unroll
                for (int mt = 0; mt < 2; ++mt) {
                    f32x4 y = t ? (mt ? aB1 : aB0) : (mt ? aA1 : aA0);
                    const int sl = 8 * qd + 32 * mt + 4 * b0 + 64 * b1;
                    v2u uu = *(LAS v2u*)(u3 + 2 * (2048 - 512 * nt - sl));
                    unsigned y01 = pk2(y[0], y[1]), y23 = pk2(y[2], y[3]);
                    unsigned p01 = pk2(bfhi(uu.y) * dd, bflo(uu.y) * dd), p23 = pk2(bfhi(uu.x) * dd, bflo(uu.x) * dd);
                    zz[t][mt].x = pk2(bflo(y01) + bflo(p01), bfhi(y01) + bfhi(p01));
                    zz[t][mt].y = pk2(bflo(y23) + bflo(p23), bfhi(y23) + bfhi(p23));
                }
            }
            v2u zo[2][2];
#pragma unroll
            for (int t = 0; t < 2; ++t) {
                CBAR();
#pragma unroll
                for (int mt = 0; mt < 2; ++mt) *(LAS v2u*)(zs + 2 * (8 * qd + 32 * mt + 4 * b0 + 64 * b1)) = zz[t][mt];
                CBAR();
                zo[t][0] = *(LAS v2u*)(zs + lane * 8); zo[t][1] = *(LAS v2u*)(zs + 512 + lane * 8);
            }
#pragma unroll
            for (int t = 0; t < 2; ++t)
#pragma unroll
                for (int i = 0; i < 2; ++i)
                    st128((v4u){zo[t][i].x, tag, zo[t][i].y, tag}, rs, WS_Z + (d0 + ch) * 8192 + (t ? half : 3 - half) * 2048 + i * 1024 + lane * 16);
            SCHED();
            lds_set(sm, F_CONV + j, lane);
        }
        ST(10);
        lds_wait(sm, F_W2OK + j, 0, lane);
        ST(11);
        v4u hbuf[7][4];
        f32x4 acc2[4][4] = {};
        HLOAD(0); HLOAD(1); HLOAD(2); HLOAD(3); HLOAD(4); HLOAD(5); HLOAD(6);
        qv = *(const GAS v4u*)(q + (size_t)(64 * rt + es) * D + 64 * ct + ed);
        SCHED();
        G2MMA(wl[0], 0);
        W2READ(wl[2], PLACE(2)); SCHED(); G2MMA(wl[1], 1);
        W2READ(wl[3], PLACE(3)); SCHED(); G2MMA(wl[2], 2);
        W2READ(wl[4], PLACE(5)); SCHED(); G2MMA(wl[3], 3);
        CBAR();
        W2READ(wl[5], PLACE(0)); SCHED(); G2MMA(wl[4], 4);
        W2READ(wl[6], PLACE(1)); SCHED(); G2MMA(wl[5], 5);
        lds_wait(sm, F_PART + j, 0, lane);
#pragma unroll
        for (int n = 0; n < 4; ++n)
#pragma unroll
            for (int s = 0; s < 4; ++s) acc2[n][s] += *(LAS f32x4*)(sm + LPIECE(n * 4 + s) + lane * 16);
        SCHED();
        G2MMA(wl[6], 6);
        ST(12);
#pragma unroll
        for (int s = 0; s < 4; ++s) {
            LAS char* pp = sm + PLACE(s) + c16 * 256;
#pragma unroll
            for (int n = 0; n < 4; ++n) *(LAS f32x4*)(pp + (((4 * n + qd) ^ c16) << 4)) = acc2[n][s];
        }
    } else {
        const int fu_off = WS_FU + (x * 32 + (lane & 31)) * 128;
        const u16* w2L = w2 + (size_t)(224 * w) * D + 64 * ct;
        const unsigned xs0 = (unsigned)((rp * 128 + 32 * j + (lane >> 3)) * D + (((lane & 7) ^ qd) * 8)) * 2u;
        const unsigned xs1 = (unsigned)((rp * 128 + 32 * j + 8 + (lane >> 3)) * D + (((lane & 7) ^ (4 + qd)) * 8)) * 2u;
        const unsigned ws0 = (unsigned)((16 * j + qd) * F + fp * 128 + (((((c16 >> 1) ^ qd) << 1) | (lane & 1)) * 8)) * 2u;
        const unsigned ws1 = (unsigned)((16 * j + 8 + qd) * F + fp * 128 + (((((c16 >> 1) ^ (qd | 4)) << 1) | (lane & 1)) * 8)) * 2u;
#define STAGE(c)                                                          \
    do {                                                                  \
        LAS char* sl = sm + ((c) % 3) * 32768 + 4096 * j;                 \
        glds16(q + (c) * 64, xs0, sl);                                    \
        glds16(w1 + (c) * 64 * F, ws0, sl + 16384);                       \
        glds16(q + (c) * 64, xs1, sl + 1024);                             \
        glds16(w1 + (c) * 64 * F + 4 * F, ws0, sl + 16384 + 1024);        \
        glds16(q + (c) * 64 + 16 * D, xs0, sl + 2048);                    \
        glds16(w1 + (c) * 64 * F, ws1, sl + 16384 + 2048);                \
        glds16(q + (c) * 64 + 16 * D, xs1, sl + 3072);                    \
        glds16(w1 + (c) * 64 * F + 4 * F, ws1, sl + 16384 + 3072);        \
    } while (0)
        {
            const char* hrow = (const char*)hk + (size_t)(d0 + ch) * 4096;
            v4u ha0, ha1; v2u hb0, hb1;
            if (isG) { STAGE(0); }
            asm volatile("global_load_dwordx4 %0, %4, %6\n\tglobal_load_dwordx2 %1, %4, %6 offset:16\n\t"
                         "global_load_dwordx4 %2, %4, %6 offset:1024\n\tglobal_load_dwordx2 %3, %5, %6"
                         : "=&v"(ha0), "=&v"(hb0), "=&v"(ha1), "=&v"(hb1)
                         : "v"(16 * L0), "v"(L1 == 255 ? 0 : 16 * L1 + 16), "s"(hrow) : "memory");
            if (isG) { STAGE(1); STAGE(2); WAIT_V(16); } else { WAIT_V(0); }
            asm volatile("" : "+v"(ha0), "+v"(hb0), "+v"(ha1), "+v"(hb1) :: "memory");
            ST(1);
            fill_pads_h(sm, ch, half, lane);
            fill_h(sm, ha0, hb0, ch, L0); fill_h(sm, ha1, hb1, ch, L1);
            if (!isG) WAIT_L0();
            SCHED();
        }
        if (isG) {
            BAR();
            ST(2);
            WAIT_V(8); BAR(); STAGE(3);
            WAIT_V(8); BAR(); STAGE(4);
            WAIT_V(8); BAR(); STAGE(5);
            ST(3);
            WAIT_V(8); BAR(); STAGE(6);
            WAIT_V(8); BAR(); STAGE(7);
            tag = __builtin_amdgcn_readfirstlane(*(volatile LAS unsigned*)(sm + L_FL + 4 * F_TAG));
            WAIT_V(8); BAR();
        } else {
            BAR(); BAR();
            tag = __builtin_amdgcn_readfirstlane(*(volatile LAS unsigned*)(sm + L_FL + 4 * F_TAG));
        }
        unsigned pr = 0;
        if (j == 0) asm volatile("global_load_dword %0, %1, %2 sc1" : "=v"(pr) : "v"(fu_off), "s"(ws) : "memory");
        W2STAGE(w2L, 4, PLACE(4)); W2STAGE(w2L, 5, PLACE(5));
        asm volatile("s_waitcnt vmcnt(8)" : "+v"(pr) :: "memory");
        if (isG) BAR();
        ST(4);
        const bool utok = j != 0 || __all(pr == tag);
        if (j == 0 && utok) lds_set(sm, F_UT, lane);
        W2STAGE(w2L, 0, PLACE(0)); W2STAGE(w2L, 1, PLACE(1));
        if (!utok) {
            poll_flags(rs, fu_off, tag);
            lds_set(sm, F_UT, lane);
        }
        if (isG) lds_wait(sm, F_C7, 3, lane);
        W2STAGE(w2L, 2, PLACE(2)); W2STAGE(w2L, 3, PLACE(3));
        s16x8 wr[6][4];
        WAIT_V(16);
        W2READ(wr[4], PLACE(4)); W2READ(wr[5], PLACE(5));
        WAIT_L0(); SCHED();
        ST(5);
        W2STAGE(w2L, 6, PLACE(4)); W2STAGE(w2C, 4, PLACE(5));
        WAIT_V(16);
        W2READ(wr[0], PLACE(0)); W2READ(wr[1], PLACE(1));
        WAIT_L0(); SCHED();
        W2STAGE(w2C, 0, PLACE(0)); W2STAGE(w2C, 1, PLACE(1));
        WAIT_V(16);
        W2READ(wr[2], PLACE(2)); W2READ(wr[3], PLACE(3));
        WAIT_L0(); SCHED();
        ST(6);
        W2STAGE(w2C, 2, PLACE(2)); W2STAGE(w2C, 3, PLACE(3));
        WAIT_V(8);
        lds_set(sm, F_C01 + j, lane);
        ST(7);
        {
            const int kpl = ((7 * (w - 4 * ((lane >> 2) & 1))) >> 1) + (lane & 3);
            poll_flags(rs, WS_FH + ((rt >> 1) * 16 + (kpl >> 1)) * 128 + ((rt & 1) + 2 * (kpl & 1)) * 4, tag);
        }
        ST(11);
        v4u hbuf[7][4];
        f32x4 acc2[4][4] = {};
        HLOAD(0); HLOAD(1); HLOAD(2); HLOAD(3);
        lds_set(sm, F_W2OK + j, lane);
        SCHED();
        G2MMA(wr[0], 0); HLOAD(4); HLOAD(5);
        G2MMA(wr[1], 1); HLOAD(6);
        qv = *(const GAS v4u*)(q + (size_t)(64 * rt + es) * D + 64 * ct + ed);
        SCHED();
        s16x8 wl[4];
        G2MMA(wr[2], 2);
        G2MMA(wr[3], 3);
        G2MMA(wr[4], 4);
        W2READ(wl, PLACE(4)); SCHED(); G2MMA(wr[5], 5);
        G2MMA(wl, 6);
        ST(12);
        const int zoff = WS_Z + (64 * ct + 16 * j + qd) * 8192 + rt * 256 + c16 * 16;
        v4u z[4]; v2u gr;
#pragma unroll
        for (int i = 0; i < 4; ++i) z[i] = ld128(rs, zoff + i * 4 * 8192);
        gr = ld64(rs, WS_G + (64 * rt + lane) * 8);
        SCHED();
        lds_wait(sm, F_CONV, 3, lane);
#pragma unroll
        for (int n = 0; n < 4; ++n)
#pragma unroll
            for (int s = 0; s < 4; ++s) *(LAS f32x4*)(sm + LPIECE(n * 4 + s) + lane * 16) = acc2[n][s];
        WAIT_L0(); SCHED();
        lds_set(sm, F_PART + j, lane);
#pragma unroll 1
        for (unsigned spins = 0; spins < SPIN_CAP; ++spins) {
            bool ok = gr.y == tag;
#pragma unroll
            for (int i = 0; i < 4; ++i) ok = ok && z[i].y == tag && z[i].w == tag;
            if (__all(ok)) break;
            __builtin_amdgcn_s_sleep(2);
            CBAR();
#pragma unroll
            for (int i = 0; i < 4; ++i) z[i] = ld128(rs, zoff + i * 4 * 8192);
            gr = ld64(rs, WS_G + (64 * rt + lane) * 8);
        }
#pragma unroll
        for (int i = 0; i < 4; ++i) *(LAS v2u*)(sm + PLACE(4) + (4 * i + qd) * 128 + c16 * 8) = (v2u){z[i].x, z[i].z};
        if (j == 0) *(LAS unsigned*)(sm + 65536 + 3072 + lane * 4) = gr.x;
        if (b == 255 && w == 5) {
#pragma unroll 1
            for (unsigned spins = 0; spins < SPIN_CAP; ++spins) {
                CBAR();
                bool ok = true;
#pragma unroll
                for (int i = 0; i < 4; ++i) ok = (ld32(rs, WS_FU + (i * 64 + lane) * 128) == tag) && ok;
                if (__all(ok)) break;
                __builtin_amdgcn_s_sleep(2);
            }
            if (lane == 0) st32(tag, rs, WS_E);
        }
    }
    WAIT_L0();
    ST(13);
    BAR();
    {
        const int zr = ed + qq;
        LAS char* zp = sm + 65536 + (zr >> 4) * 8192 + (zr & 15) * 128 + 32 * j + 8 * p4;
        v2u za = __builtin_bit_cast(v2u, trread(zp)), zb = __builtin_bit_cast(v2u, trread(zp + 512));
        const float gg = *(LAS float*)(sm + 65536 + 3072 + es * 4);
        LAS char* pp = sm + (j >> 1) * 32768 + (j & 1) * 4096 + c16 * 256;
        const int sl0 = ((ed >> 2) ^ c16) << 4, sl1 = (((ed >> 2) + 1) ^ c16) << 4;
        f32x4 s0 = *(LAS f32x4*)(pp + sl0), s1 = *(LAS f32x4*)(pp + sl1);
#pragma unroll
        for (int i = 1; i < 4; ++i) { s0 += *(LAS f32x4*)(pp + i * 8192 + sl0); s1 += *(LAS f32x4*)(pp + i * 8192 + sl1); }
        const float g1 = rbf(1.0f - gg);
        unsigned zw[4] = {za.x, za.y, zb.x, zb.y};
        float bs[8] = {s0[0], s0[1], s0[2], s0[3], s1[0], s1[1], s1[2], s1[3]};
        v4u o;
#pragma unroll
        for (int e = 0; e < 4; ++e) {
            unsigned bb = pk2(bs[2 * e], bs[2 * e + 1]);
            unsigned aa = pk2(bflo(qv[e]) * bflo(zw[e]), bfhi(qv[e]) * bfhi(zw[e]));
            unsigned ga = pk2(gg * bflo(aa), gg * bfhi(aa));
            unsigned gb = pk2(g1 * bflo(bb), g1 * bfhi(bb));
            o[e] = pk2(bflo(ga) + bflo(gb), bfhi(ga) + bfhi(gb));
        }
        *(GAS v4u*)(out + (size_t)(64 * rt + es) * D + 64 * ct + ed) = o;
    }
    asm volatile(".Lfused_end:" ::: "memory");
    ST(14);
    ST(15);
    ST_END(isG ? 0 : 1);
}

extern "C" void kernel_launch(void* const* d_in, const int* in_sizes, int n_in, void* d_out, int out_size, void* d_ws, size_t ws_size,
                              hipStream_t stream) {
    const u16* q = (const u16*)d_in[0]; const u16* k = (const u16*)d_in[1]; const u16* v = (const u16*)d_in[2];
    const u16* sk = (const u16*)d_in[3]; const u16* hk = (const u16*)d_in[4]; const u16* Dv = (const u16*)d_in[5];
    const u16* w1 = (const u16*)d_in[7]; const u16* w2 = (const u16*)d_in[8]; const u16* gw = (const u16*)d_in[9];
    fused<<<256, 512, 0, stream>>>(q, k, v, sk, hk, Dv, w1, w2, gw, (char*)d_ws, (u16*)d_out);
}
```

```cpp
#include <hip/hip_runtime.h>
#include <hip/hip_bf16.h>
#include <stdint.h>

#define LAS __attribute__((address_space(3)))
#define GAS __attribute__((address_space(1)))
typedef unsigned short u16;
typedef short s16x4 __attribute__((ext_vector_type(4)));
typedef short s16x8 __attribute__((ext_vector_type(8)));
typedef float f32x4 __attribute__((ext_vector_type(4)));
typedef unsigned v4u __attribute__((ext_vector_type(4)));
typedef unsigned v2u __attribute__((ext_vector_type(2)));
typedef __bf16 bf16x2_t __attribute__((ext_vector_type(2)));
typedef float f32x2 __attribute__((ext_vector_type(2)));
typedef __amdgpu_buffer_rsrc_t rsrc_t;

constexpr int S = 2048, D = 512, F = 1792;
#define PFA 3
#define PFB 4
#define HK2V 24
#define HKV 20

constexpr int WS_HF = 0;
constexpr int WS_UT = 8 << 20;
constexpr int WS_Z = 10 << 20;
constexpr int WS_G = 14 << 20;
constexpr int WS_FU = WS_G + 65536;
constexpr int WS_FH = WS_FU + 32768;
constexpr int WS_E = WS_FH + 32768;
constexpr int WS_BYTES = 16 << 20;
constexpr int SC1 = 16;
constexpr unsigned SPIN_CAP = 1u << 16;

__device__ __forceinline__ float bflo(unsigned w) { return __uint_as_float(w << 16); }
__device__ __forceinline__ float bfhi(unsigned w) { return __uint_as_float(w & 0xFFFF0000u); }
__device__ __forceinline__ unsigned pk2v(f32x2 f) { return __builtin_bit_cast(unsigned, __builtin_convertvector(f, bf16x2_t)); }
__device__ __forceinline__ unsigned pk2(float lo, float hi) { return pk2v((f32x2){lo, hi}); }
__device__ __forceinline__ float rbf(float f) { return bflo(pk2(f, f)); }
__device__ __forceinline__ s16x4 trread(LAS char* p) { return __builtin_amdgcn_ds_read_tr16_b64_v4i16((LAS s16x4*)p); }
__device__ __forceinline__ s16x8 cat8(s16x4 a, s16x4 b) { return __builtin_shufflevector(a, b, 0, 1, 2, 3, 4, 5, 6, 7); }
__device__ __forceinline__ void glds16(const void* base, unsigned voff, LAS char* l) {
    asm volatile("s_mov_b32 m0, %2\n\ts_nop 0\n\tglobal_load_lds_dwordx4 %0, %1"
                 :: "v"(voff), "s"(base), "s"((unsigned)(size_t)l) : "memory");
}
__device__ __forceinline__ void glds16x4(const void* base, unsigned vo0, unsigned vo1, unsigned vo2, unsigned vo3, LAS char* l) {
    asm volatile("s_mov_b32 m0, %5\n\ts_nop 0\n\tglobal_load_lds_dwordx4 %0, %4\n\tglobal_load_lds_dwordx4 %1, %4 offset:1024\n\t"
                 "global_load_lds_dwordx4 %2, %4 offset:2048\n\tglobal_load_lds_dwordx4 %3, %4 offset:3072"
                 :: "v"(vo0), "v"(vo1), "v"(vo2), "v"(vo3), "s"(base), "s"((unsigned)(size_t)l) : "memory");
}
#define WAIT_V(n) asm volatile("s_waitcnt vmcnt(" #n ")" ::: "memory")
#define WAIT_V0C() __builtin_amdgcn_s_waitcnt(0x0F70)
#define WAIT_L0() __builtin_amdgcn_s_waitcnt(0xC07F)
#define BAR() asm volatile("s_barrier" ::: "memory")
#define SCHED() __builtin_amdgcn_sched_barrier(0)
#define CBAR() asm volatile("" ::: "memory")
#define ST_DECL
#define ST(i)
#define ST_END(k)
#define STF(i)
#define MFMA(a, b, c) __builtin_amdgcn_mfma_f32_16x16x32_bf16(a, b, c, 0, 0, 0)

__device__ __forceinline__ v4u ld128(rsrc_t rs, int off) { return __builtin_amdgcn_raw_buffer_load_b128(rs, off, 0, SC1); }
__device__ __forceinline__ v2u ld64(rsrc_t rs, int off) { return __builtin_amdgcn_raw_buffer_load_b64(rs, off, 0, SC1); }
__device__ __forceinline__ unsigned ld32(rsrc_t rs, int off) { return __builtin_amdgcn_raw_buffer_load_b32(rs, off, 0, SC1); }
__device__ __forceinline__ void st128(v4u v, rsrc_t rs, int off) { __builtin_amdgcn_raw_buffer_store_b128(v, rs, off, 0, SC1); }
__device__ __forceinline__ void st64(v2u v, rsrc_t rs, int off) { __builtin_amdgcn_raw_buffer_store_b64(v, rs, off, 0, SC1); }
__device__ __forceinline__ void st32(unsigned v, rsrc_t rs, int off) { __builtin_amdgcn_raw_buffer_store_b32(v, rs, off, 0, SC1); }
__device__ __forceinline__ void poll_flags(rsrc_t rs, int off, unsigned tag) {
#pragma unroll 1
    for (unsigned spins = 0; spins < SPIN_CAP; ++spins) {
        CBAR();
        unsigned v = ld32(rs, off);
        if (__all(v == tag)) break;
        __builtin_amdgcn_s_sleep(4);
    }
    CBAR();
}
__device__ __forceinline__ v4u silu8(f32x4 a0, f32x4 a1) {
    f32x2 x[4] = {{a0[0], a0[1]}, {a0[2], a0[3]}, {a1[0], a1[1]}, {a1[2], a1[3]}};
    f32x2 t[4];
#pragma unroll
    for (int i = 0; i < 4; ++i) t[i] = x[i] * -1.44269504088896f;
#pragma unroll
    for (int i = 0; i < 4; ++i) { t[i].x = __builtin_amdgcn_exp2f(t[i].x); t[i].y = __builtin_amdgcn_exp2f(t[i].y); }
#pragma unroll
    for (int i = 0; i < 4; ++i) t[i] = t[i] + 1.0f;
#pragma unroll
    for (int i = 0; i < 4; ++i) { t[i].x = __builtin_amdgcn_rcpf(t[i].x); t[i].y = __builtin_amdgcn_rcpf(t[i].y); }
    v4u o;
#pragma unroll
    for (int i = 0; i < 4; ++i) o[i] = pk2v(x[i] * t[i]);
    return o;
}

constexpr int L_CV = 98304;
constexpr int L_KV = 131072;
constexpr int L_T = L_KV + 22528;
constexpr int U_CP = 4416, U_CH = 4 * U_CP, H_CP = 6400, H_CH = 2 * H_CP;
constexpr int L_U = L_CV, L_H = L_CV + 2 * U_CH;
constexpr int LDS_BYTES = 163840;
constexpr int L_ZS = L_H + 2 * H_CH;
constexpr int L_FL = LDS_BYTES - 128;
enum { F_W2OK = 0, F_C7 = 4, F_FILL = 8, F_CONV = 12, F_C01 = 16, F_PART = 20, F_TAG = 24, F_UT = 25 };

__device__ __forceinline__ void lds_set(LAS char* sm, int i, int lane) {
    if (lane == 0) *(volatile LAS unsigned*)(sm + L_FL + 4 * i) = 1u;
}
__device__ __forceinline__ void lds_wait(LAS char* sm, int i0, int mask, int lane) {
#pragma unroll 1
    for (unsigned spins = 0; spins < (1u << 22); ++spins) {
        unsigned v = *(volatile LAS unsigned*)(sm + L_FL + 4 * (i0 + (lane & mask)));
        if (__all(v != 0u)) break;
        __builtin_amdgcn_s_sleep(1);
    }
    CBAR();
}

__device__ __forceinline__ v4u kv_prep(LAS char* kvw, int e, int lane, bool zero_halo) {
    const int gq = lane >> 4, qq = (lane & 15) >> 2, p = lane & 3;
    LAS char* kb = kvw + (8 * e + 4 + qq) * 128 + 32 * gq + 8 * p;
    v2u k0 = __builtin_bit_cast(v2u, trread(kb));
    v2u k1 = __builtin_bit_cast(v2u, trread(kb + 512));
    v2u k2 = __builtin_bit_cast(v2u, trread(kb + 1024));
    LAS char* vb = kvw + 3072 + (8 * e + qq) * 128 + 32 * gq + 8 * p;
    v2u v0 = __builtin_bit_cast(v2u, trread(vb));
    v2u v1 = __builtin_bit_cast(v2u, trread(vb + 512));
    v2u sk = *(LAS v2u*)(kvw + 5120 + 8 * lane);
    if (zero_halo) { k0.x = 0; k0.y = 0; }
    float kk[12] = {bflo(k0.x), bfhi(k0.x), bflo(k0.y), bfhi(k0.y), bflo(k1.x), bfhi(k1.x), bflo(k1.y), bfhi(k1.y),
                    bflo(k2.x), bfhi(k2.x), bflo(k2.y), bfhi(k2.y)};
    float vv[8] = {bflo(v0.x), bfhi(v0.x), bflo(v0.y), bfhi(v0.y), bflo(v1.x), bfhi(v1.x), bflo(v1.y), bfhi(v1.y)};
    const float w0 = bflo(sk.x), w1 = bfhi(sk.x), w2 = bflo(sk.y), w3 = bfhi(sk.y);
    float ks[8];
#pragma unroll
    for (int i = 0; i < 8; ++i) ks[i] = kk[i + 1] * w3 + kk[i + 2] * w2 + kk[i + 3] * w1 + kk[i + 4] * w0;
    float u[8];
#pragma unroll
    for (int i = 0; i < 8; i += 2) {
        unsigned r = pk2(ks[i], ks[i + 1]);
        u[i] = bflo(r) * vv[i];
        u[i + 1] = bfhi(r) * vv[i + 1];
    }
    return (v4u){pk2(u[7], u[6]), pk2(u[5], u[4]), pk2(u[3], u[2]), pk2(u[1], u[0])};
}

__device__ __forceinline__ unsigned ab16(unsigned hi, unsigned lo) { return __builtin_amdgcn_alignbit(hi, lo, 16); }
__device__ __forceinline__ void fill_u(LAS char* sm, v2u ua, v4u ubv, int ch, int L) {
    if (L == 0) { ua.x = 0; ua.y = 0; }
    const unsigned w0 = ua.x, w1 = ua.y, w2 = ubv.x, w3 = ubv.y, w4 = ubv.z, w5 = ubv.w;
    const unsigned t0 = ab16(w1, w0), t1 = ab16(w2, w1), t2 = ab16(w3, w2), t3 = ab16(w4, w3), t4 = ab16(w5, w4);
    LAS char* ub = sm + L_U + ch * U_CH;
    LAS char* up = ub + 16 * L + 64;
    *(LAS v4u*)(up) = (v4u){t1, t2, t3, t4};
    *(LAS v4u*)(up + U_CP) = (v4u){w1, w2, w3, w4};
    *(LAS v4u*)(up + 2 * U_CP) = (v4u){t0, t1, t2, t3};
    *(LAS v4u*)(up + 3 * U_CP) = (v4u){w0, w1, w2, w3};
    if (L == 255) {
        LAS char* e = ub + 16 * 256 + 64;
        *(LAS v4u*)(e) = (v4u){w5 >> 16, 0, 0, 0};
        *(LAS v4u*)(e + U_CP) = (v4u){w5, 0, 0, 0};
        *(LAS v4u*)(e + 2 * U_CP) = (v4u){t4, w5 >> 16, 0, 0};
        *(LAS v4u*)(e + 3 * U_CP) = (v4u){w4, w5, 0, 0};
    }
}
__device__ __forceinline__ void fill_h(LAS char* sm, v4u ha, v2u hbv, int ch, int L) {
    if (L == 255) { hbv.x = 0; hbv.y = 0; }
    LAS char* hb = sm + L_H + ch * H_CH;
    LAS char* hp = hb + ((L >> 3) + 8) * 160 + (L & 7) * 16;
    *(LAS v4u*)(hp) = ha;
    *(LAS v4u*)(hp + H_CP) = (v4u){ha.z, ha.w, hbv.x, hbv.y};
    if (L == 0) *(LAS v4u*)(hb + H_CP + 7 * 160 + 7 * 16) = (v4u){0, 0, ha.x, ha.y};
}
__device__ __forceinline__ void fill_pads_h(LAS char* sm, int ch, int half, int lane) {
    const v4u z = {0, 0, 0, 0};
    LAS char* hb = sm + L_H + ch * H_CH;
    if (!(half == 1 && lane == 63)) *(LAS v4u*)(hb + half * H_CP + (lane >> 3) * 160 + (lane & 7) * 16) = z;
}
__device__ __forceinline__ void fill_pads_u(LAS char* sm, int ch, int half, int lane) {
    const v4u z = {0, 0, 0, 0};
    if (lane < 16) {
        LAS char* ub = sm + L_U + ch * U_CH;
        const int c = 2 * half + (lane >> 3), jx = lane & 7;
        if (jx != 4) *(LAS v4u*)(ub + c * U_CP + (jx < 4 ? 16 * jx : 16 * (252 + jx) + 64)) = z;
    }
}

constexpr int conv_want(int t, int T0, int NA) {
    int wnt = (t + PFA < T0) ? t + PFA : (t + PFB > T0 - 1 ? t + PFB : T0 - 1);
    return t < -1 ? -1 : (wnt < NA - 1 ? wnt : NA - 1);
}
template <int I> struct IC { static constexpr int v = I; };
template <int A, int B, class Fn> __device__ __forceinline__ void static_for(Fn&& fn) {
    if constexpr (A < B) { fn(IC<A>{}); static_for<A + 1, B>(fn); }
}
template <int NTA, int NTB, class Hook>
__device__ __forceinline__ void conv_body(LAS char* ua, LAS char* hb, f32x4& aA0, f32x4& aA1, f32x4& aB0, f32x4& aB1, Hook hook) {
    constexpr int NA = 16 * NTA + 17, NB = 16 * NTB + 17, T0 = NA - NB;
    constexpr int HK = HKV, HK2 = HK2V;
    LAS char* up = ua - 64 * (NA - 1);
    asm volatile("" : "+v"(up), "+v"(hb));
    s16x8 W[NA + 1], BA[NA], BB[NB];
#define HOFF(t) (((t) & 1) ? (((t) + 1) / 2) * 160 : ((t) / 2) * 160 + 64)
    W[0] = *(LAS s16x8*)up;
    constexpr int PF = PFA;
    static_for<0, PF>([&](auto nc) __attribute__((always_inline)) {
        constexpr int nl = decltype(nc)::v;
        W[nl + 1] = *(LAS s16x8*)(up + 64 * (nl + 1));
        BA[nl] = *(LAS s16x8*)(hb + HOFF(nl));
    });
    SCHED();
    static_for<0, NA>([&](auto tc) __attribute__((always_inline)) {
        constexpr int t = decltype(tc)::v, nl = t + PF;
        aA1 = MFMA(W[t], BA[t], aA1);
        SCHED();
        if constexpr (nl + 1 < NA) W[nl + 1] = *(LAS s16x8*)(up + 64 * (nl + 1));
        SCHED();
        if constexpr (t + 1 < NA) aA0 = MFMA(W[t + 1], BA[t], aA0);
        SCHED();
        if constexpr (nl < NA) BA[nl] = *(LAS s16x8*)(hb + HOFF(nl));
        SCHED();
        if constexpr (t >= T0) { aB1 = MFMA(W[t], BB[t - T0], aB1); SCHED(); }
        if constexpr (nl >= T0 && nl < NA) { BB[nl - T0] = *(LAS s16x8*)(hb + HOFF(nl - T0)); SCHED(); }
        if constexpr (t >= T0 && t + 1 < NA) { aB0 = MFMA(W[t + 1], BB[t - T0], aB0); SCHED(); }
        if constexpr (t == HK) { hook(-1); SCHED(); }
        if constexpr (t == HK2) { hook(0); SCHED(); }
    });
}

__global__ __launch_bounds__(512, 2) void fused(const u16* __restrict__ q, const u16* __restrict__ kin, const u16* __restrict__ vin,
                                                const u16* __restrict__ sk, const u16* __restrict__ hk, const u16* __restrict__ Dv,
                                                const u16* __restrict__ w1, const u16* __restrict__ w2, const u16* __restrict__ gw,
                                                char* __restrict__ ws, u16* __restrict__ out) {
    __shared__ __attribute__((aligned(16))) char smem[LDS_BYTES];
    LAS char* sm = (LAS char*)smem;
    const int b = blockIdx.x, x = b & 7, idx = b >> 3;
    const int tid = threadIdx.x, lane = tid & 63;
    const int w = __builtin_amdgcn_readfirstlane(tid >> 6);
    const int j = w & 3;
    const rsrc_t rs = __builtin_amdgcn_make_buffer_rsrc(ws, 0, WS_BYTES, 0x00020000);
    ST_DECL; ST(0);
    unsigned long long pc; unsigned len;
    asm volatile(".Lfused_pc:\n\ts_getpc_b64 %0\n\ts_mov_b32 %1, .Lfused_end-.Lfused_pc" : "=s"(pc), "=s"(len));
    const int rt = 4 * x + (idx & 3), ct = idx >> 2;
    const int d0 = 64 * x + 2 * idx;
    const int c16 = lane & 15, qd = lane >> 4, qq = c16 >> 2, p4 = lane & 3;
    const bool isG = idx < 28;
    const int rp = 4 * (x >> 1) + (idx & 3), fp = 7 * (x & 1) + (idx >> 2);
    const int ch = j >> 1, half = j & 1;
    const int L0 = 128 * half + lane, L1 = L0 + 64;

#define PLACE(i) (((i) >> 1) * 32768 + j * 8192 + ((i) & 1) * 4096)
    const int w2rd = (4 * qd + qq) * 128 + 32 * ((2 * qd + (qq >> 1)) & 3) + 8 * p4;
#define W2READ(dst, addr)                                                                    \
    do {                                                                                     \
        _Pragma("unroll") for (int n_ = 0; n_ < 4; ++n_) {                                   \
            LAS char* a_ = sm + (addr) + (w2rd ^ (n_ << 5));                                 \
            dst[n_] = cat8(trread(a_), trread(a_ + 2048));                                   \
        }                                                                                    \
    } while (0)
    const unsigned w2off = (unsigned)((lane >> 3) * D + ((((((lane & 7) >> 1) ^ qd) << 1) | (lane & 1)) * 8)) * 2u;
    const u16* w2C = w2 + (size_t)(224 * j) * D + 64 * ct;
#define W2STAGE(base, k_, addr) glds16x4(base + (32 * (k_)) * D, w2off, w2off + 7168u, w2off + 14336u, w2off + 21504u, sm + (addr))
    const int h_off = WS_HF + (rt * 56 + 7 * w) * 4096 + lane * 16;
    const int es = 16 * j + c16, ed = 32 * (w >> 2) + 8 * qd;
    unsigned tag;
    v4u qv;
#define HLOAD(k_) do { _Pragma("unroll") for (int i_ = 0; i_ < 4; ++i_) hbuf[k_][i_] = ld128(rs, h_off + (k_) * 4096 + i_ * 1024); SCHED(); } while (0)
#define G2MMA(wf, k_) do { _Pragma("unroll") for (int n_ = 0; n_ < 4; ++n_) _Pragma("unroll") for (int s_ = 0; s_ < 4; ++s_) \
        acc2[n_][s_] = MFMA(wf[n_], __builtin_bit_cast(s16x8, hbuf[k_][s_]), acc2[n_][s_]); SCHED(); } while (0)
#define LPIECE(i) ((i) == 15 ? PLACE(4) + 2048 : L_CV + j * 16384 + (i) * 1024)

    if (w < 4) {
        unsigned ev, dvr, tch, dvs;
        LAS char* kvw = sm + L_KV + j * 5632;
        {
            const int r_ = 64 * idx + 16 * j - 8 + (lane >> 3);
            const unsigned co_ = (unsigned)(64 * x + (lane & 7) * 8) * 2u;
            glds16(kin, (unsigned)((r_ < 0 ? 0 : r_) * D) * 2u + co_, kvw);
            const unsigned o1_ = (unsigned)((r_ + 8) * D) * 2u + co_;
            glds16(kin, o1_, kvw + 1024);
            glds16(vin, o1_, kvw + 3072);
            if (lane < 32) glds16(sk, (unsigned)(256 * x + lane * 8) * 2u, kvw + 5120);
            glds16(kin + 8 * D, o1_, kvw + 2048);
            glds16(vin + 8 * D, o1_, kvw + 4096);
        }
        asm volatile("global_load_dword %0, %2, %3 sc1\n\tglobal_load_ushort %1, %2, %4" : "=&v"(ev), "=&v"(dvr) : "v"(0), "s"(ws + WS_E), "s"(Dv + d0 + ch) : "memory");
        if (w == 0 && lane < 32 && lane != F_TAG) *(LAS unsigned*)(sm + L_FL + 4 * lane) = 0u;
        const int sh = j & 1, fh = j >> 1;
        LAS char* bad[2][2]; LAS char* aad[2][4];
        {
            const int key = qq | ((qd & 1) << 2);
#pragma unroll
            for (int sp = 0; sp < 2; ++sp) {
                bad[sp][0] = sm + sp * 65536 + (64 * sh + c16) * 128 + 16 * (qd ^ (c16 >> 1));
                bad[sp][1] = sm + sp * 65536 + (64 * sh + c16) * 128 + 16 * ((4 + qd) ^ (c16 >> 1));
#pragma unroll
                for (int ft = 0; ft < 4; ++ft) aad[sp][ft] = sm + sp * 65536 + 16384 + (8 * qd + qq) * 256 + 32 * ((4 * fh + ft) ^ key) + 8 * p4;
                asm volatile("" : "+v"(bad[sp][0]), "+v"(bad[sp][1]), "+v"(aad[sp][0]), "+v"(aad[sp][1]), "+v"(aad[sp][2]), "+v"(aad[sp][3]));
            }
        }
        LAS char* tsrc = sm + L_T + (16 * j + (lane >> 3)) * 144 + (lane & 7) * 16;
        int tdst = WS_UT + (64 * x + 16 * j + (lane >> 3)) * 4096 + (1984 - 64 * idx) * 2 + (lane & 7) * 16;
        asm volatile("" : "+v"(tsrc), "+v"(tdst));
        WAIT_V(4);
        ST(1);
        {
            const char* tb; unsigned to;
            const int fpc = fp < 14 ? fp : 13, ixc = isG ? idx : 0;
            if (j == 0) { const int id = 128 + fpc * 64 + lane; tb = (const char*)q; to = (unsigned)((rp * 128 + (id & 127)) * 1024 + (id >> 7) * 128); }
            else if (j == 1) { tb = (const char*)w1; to = (unsigned)((32 * rp + (lane >> 1)) * (F * 2) + fpc * 256 + (lane & 1) * 128); }
            else if (j == 2) { tb = (const char*)w2; to = (unsigned)((16 * ((x >> 1) * 28 + ixc) + (lane >> 2)) * 1024 + (x & 1) * 512 + (lane & 3) * 128); }
            else { tb = (const char*)pc; to = (unsigned)(idx * 16 + c16) * 128u; to = to < len ? to : 0u; }
            asm volatile("global_load_dword %0, %1, %2" : "=v"(tch) : "v"(to), "s"(tb) : "memory");
        }

        *(LAS v4u*)(sm + L_T + lane * 144 + (7 - 2 * j) * 16) = kv_prep(kvw, 0, lane, idx == 0 && j == 0);
        SCHED(); WAIT_V(3);
        *(LAS v4u*)(sm + L_T + lane * 144 + (6 - 2 * j) * 16) = kv_prep(kvw, 1, lane, false);
        WAIT_L0(); SCHED();
        ST(2);
        BAR();
#define UT_STORE() do { v4u o0_ = *(LAS v4u*)tsrc, o1_ = *(LAS v4u*)(tsrc + 8 * 144); st128(o0_, rs, tdst); st128(o1_, rs, tdst + 8 * 4096); } while (0)
#define GET_TAG() do { \
            asm volatile("s_waitcnt vmcnt(0)\n\tv_readfirstlane_b32 %0, %2\n\tv_readfirstlane_b32 %1, %3\n\ts_nop 3" : "=s"(tag), "=s"(dvs) : "v"(ev), "v"(dvr), "v"(tch) : "memory"); \
            tag += 1u; \
            if (tid == 0) *(LAS unsigned*)(sm + L_FL + 4 * F_TAG) = tag; } while (0)
        const int fu_st = WS_FU + (x * 32 + idx) * 128;
        if (isG) {
            f32x4 acc[4][4] = {};
            s16x8 fa[2][2][4], fb[2][2][4];
#define READ(c)                                                                                     \
    do {                                                                                            \
        _Pragma("unroll") for (int kk = 0; kk < 2; ++kk) {                                          \
            _Pragma("unroll") for (int ft = 0; ft < 4; ++ft) {                                      \
                fb[(c) & 1][kk][ft] = *(LAS s16x8*)(bad[((c) >> 1) & 1][kk] + ((c) & 1) * 32768 + 2048 * ft); \
                LAS char* a_ = aad[((c) >> 1) & 1][ft] + ((c) & 1) * 32768 + kk * 8192;             \
                fa[(c) & 1][kk][ft] = cat8(trread(a_), trread(a_ + 1024));                          \
            }                                                                                       \
        }                                                                                           \
    } while (0)
#define MMA(c)                                                                                      \
    do {                                                                                            \
        _Pragma("unroll") for (int kk = 0; kk < 2; ++kk)                                            \
            _Pragma("unroll") for (int ft = 0; ft < 4; ++ft)                                        \
                _Pragma("unroll") for (int st = 0; st < 4; ++st)                                    \
                    acc[ft][st] = MFMA(fa[(c) & 1][kk][ft], fb[(c) & 1][kk][st], acc[ft][st]);      \
    } while (0)
#define ILV() do { _Pragma("unroll") for (int g_ = 0; g_ < 24; ++g_) { __builtin_amdgcn_sched_group_barrier(0x100, 1, 0); __builtin_amdgcn_sched_group_barrier(0x8, 1, 0); } \
                   __builtin_amdgcn_sched_group_barrier(0x8, 8, 0); SCHED(); } while (0)
            STF(1); SCHED(); UT_STORE(); READ(0); SCHED(); WAIT_L0(); STF(2);
            BAR(); STF(3); SCHED(); READ(1); MMA(0); ILV(); WAIT_L0(); STF(4);
            BAR(); STF(5); SCHED(); READ(2); MMA(1); ILV(); STF(6);
            GET_TAG(); WAIT_L0(); STF(7);
            BAR(); STF(8);
            if (tid == 0) st32(tag, rs, fu_st);
            SCHED(); READ(3); MMA(2); ILV(); WAIT_L0(); STF(9);
            ST(3);
            BAR(); STF(10); SCHED(); READ(4); MMA(3); ILV(); WAIT_L0(); STF(11);
            BAR(); STF(12); SCHED(); READ(5); MMA(4); ILV(); WAIT_L0(); STF(13);
            BAR(); STF(14); SCHED(); READ(6); MMA(5); ILV(); WAIT_L0();
            BAR(); STF(15); SCHED();
            ST(4);
            READ(7); MMA(6); ILV(); WAIT_L0();
            lds_set(sm, F_C7 + j, lane);
            SCHED();
            MMA(7);
            ST(5);
            const int hst = WS_HF + ((2 * rp + sh) * 56 + 4 * fp + 2 * fh) * 4096 + lane * 16;
#pragma unroll
            for (int pp = 0; pp < 2; ++pp)
#pragma unroll
                for (int st = 0; st < 4; ++st) st128(silu8(acc[2 * pp][st], acc[2 * pp + 1][st]), rs, hst + pp * 4096 + st * 1024);
            ST(6);
        } else {
            UT_STORE();
            const int r0 = 512 * (x >> 1) + 64 * (4 * (x & 1) + (idx - 28)) + 16 * j;
            v4u gv = *(const GAS v4u*)(gw + lane * 8);
            float gs[2];
#pragma unroll 1
            for (int hh = 0; hh < 2; ++hh) {
                v4u qr[8];
#pragma unroll
                for (int i = 0; i < 8; ++i) qr[i] = *(const GAS v4u*)(q + (size_t)(r0 + 8 * hh + i) * D + lane * 8);
                if (hh == 0) {
                    GET_TAG(); WAIT_L0();
                    BAR();
                    if (tid == 0) st32(tag, rs, fu_st);
                }
                float s[8];
#pragma unroll
                for (int i = 0; i < 8; ++i) {
                    s[i] = 0.f;
#pragma unroll
                    for (int e = 0; e < 4; ++e) {
                        unsigned r = pk2(bflo(qr[i][e]) * bflo(gv[e]), bfhi(qr[i][e]) * bfhi(gv[e]));
                        s[i] += bflo(r) + bfhi(r);
                    }
                }
#pragma unroll
                for (int i = 0; i < 4; ++i) { const bool hi = lane & 32; float snd = hi ? s[i] : s[i + 4], kp = hi ? s[i + 4] : s[i]; s[i] = kp + __shfl_xor(snd, 32); }
#pragma unroll
                for (int i = 0; i < 2; ++i) { const bool hi = lane & 16; float snd = hi ? s[i] : s[i + 2], kp = hi ? s[i + 2] : s[i]; s[i] = kp + __shfl_xor(snd, 16); }
                { const bool hi = lane & 8; float snd = hi ? s[0] : s[1], kp = hi ? s[1] : s[0]; s[0] = kp + __shfl_xor(snd, 8); }
                s[0] += __shfl_xor(s[0], 4); s[0] += __shfl_xor(s[0], 2); s[0] += __shfl_xor(s[0], 1);
                if (hh == 0) gs[0] = s[0]; else gs[1] = s[0];
            }
            if ((lane & 7) < 2) {
                float t = rbf((lane & 1) ? gs[1] : gs[0]);
                float g = rbf(__builtin_amdgcn_rcpf(1.0f + __builtin_amdgcn_exp2f(-1.44269504088896f * t)));
                st64((v2u){__float_as_uint(g), tag}, rs, WS_G + (r0 + 8 * (lane & 1) + (lane >> 3)) * 8);
            }
        }
#define H_PUBLISH() do { WAIT_V(0); if (isG && lane == 0) st32(tag, rs, WS_FH + (rp * 16 + fp) * 128 + j * 4); ST(8); } while (0)
        const bool fill_ready = __all(*(volatile LAS unsigned*)(sm + L_FL + 4 * (F_FILL + 2 * ch + (lane & 1))) != 0u);
        if (!fill_ready) {
            H_PUBLISH();
            lds_wait(sm, F_FILL + 2 * ch, 1, lane);
        }
        CBAR();
        ST(7);
        s16x8 wl[7][4];
        f32x4 aA0 = {}, aA1 = {}, aB0 = {}, aB1 = {};
        {
            LAS char* ua_ = sm + L_U + ch * U_CH + (c16 & 3) * U_CP + 64 + 4096 + 16 * (qd - (c16 >> 2));
            LAS char* hb_ = sm + L_H + ch * H_CH + (c16 >> 3) * H_CP + (c16 & 7) * 160 + qd * 16;
            auto hook = [&](int i) __attribute__((always_inline)) {
                if (i < 0) {
                    if (fill_ready) H_PUBLISH();
                } else {
                    lds_wait(sm, F_C01 + j, 0, lane);
                    W2READ(wl[0], PLACE(0)); W2READ(wl[1], PLACE(1));
                    WAIT_L0();
                    lds_set(sm, F_PART + j, lane);
                }
            };
            if (half == 0) conv_body<3, 0>(ua_, hb_, aA0, aA1, aB0, aB1, hook);
            else conv_body<2, 1>(ua_, hb_, aA0, aA1, aB0, aB1, hook);
        }
        ST(9);
        {
            const float dd = bflo(dvs);
            const int b0 = c16 >> 3, b1 = c16 & 7;
            LAS char* u3 = sm + L_U + ch * U_CH + 3 * U_CP + 64;
            LAS char* zs = sm + L_ZS + j * 1024;
            v2u zz[2][2];
#pragma unroll
            for (int t = 0; t < 2; ++t) {
                const int nt = t ? half : 3 - half;
#pragma unroll
                for (int mt = 0; mt < 2; ++mt) {
                    f32x4 y = t ? (mt ? aB1 : aB0) : (mt ? aA1 : aA0);
                    const int sl = 8 * qd + 32 * mt + 4 * b0 + 64 * b1;
                    v2u uu = *(LAS v2u*)(u3 + 2 * (2048 - 512 * nt - sl));
                    unsigned y01 = pk2(y[0], y[1]), y23 = pk2(y[2], y[3]);
                    unsigned p01 = pk2(bfhi(uu.y) * dd, bflo(uu.y) * dd), p23 = pk2(bfhi(uu.x) * dd, bflo(uu.x) * dd);
                    zz[t][mt].x = pk2(bflo(y01) + bflo(p01), bfhi(y01) + bfhi(p01));
                    zz[t][mt].y = pk2(bflo(y23) + bflo(p23), bfhi(y23) + bfhi(p23));
                }
            }
            v2u zo[2][2];
#pragma unroll
            for (int t = 0; t < 2; ++t) {
                CBAR();
#pragma unroll
                for (int mt = 0; mt < 2; ++mt) *(LAS v2u*)(zs + 2 * (8 * qd + 32 * mt + 4 * b0 + 64 * b1)) = zz[t][mt];
                CBAR();
                zo[t][0] = *(LAS v2u*)(zs + lane * 8); zo[t][1] = *(LAS v2u*)(zs + 512 + lane * 8);
            }
#pragma unroll
            for (int t = 0; t < 2; ++t)
#pragma unroll
                for (int i = 0; i < 2; ++i)
                    st128((v4u){zo[t][i].x, tag, zo[t][i].y, tag}, rs, WS_Z + (d0 + ch) * 8192 + (t ? half : 3 - half) * 2048 + i * 1024 + lane * 16);
            SCHED();
            lds_set(sm, F_CONV + j, lane);
        }
        ST(10);
        lds_wait(sm, F_W2OK + j, 0, lane);
        ST(11);
        v4u hbuf[7][4];
        f32x4 acc2[4][4] = {};
        HLOAD(0); HLOAD(1); HLOAD(2); HLOAD(3);
        G2MMA(wl[0], 0);
        HLOAD(4); HLOAD(5); HLOAD(6);
        qv = *(const GAS v4u*)(q + (size_t)(64 * rt + es) * D + 64 * ct + ed);
        SCHED();
        W2READ(wl[2], PLACE(2)); SCHED(); G2MMA(wl[1], 1);
        W2READ(wl[3], PLACE(3)); SCHED(); G2MMA(wl[2], 2);
        W2READ(wl[4], PLACE(5)); SCHED(); G2MMA(wl[3], 3);
        W2READ(wl[5], PLACE(0)); SCHED(); G2MMA(wl[4], 4);
        W2READ(wl[6], PLACE(1)); SCHED(); G2MMA(wl[5], 5);
        G2MMA(wl[6], 6);
        ST(12);
#pragma unroll
        for (int s = 0; s < 4; ++s) {
            LAS char* pp = sm + PLACE(s) + c16 * 256;
#pragma unroll
            for (int n = 0; n < 4; ++n) *(LAS f32x4*)(pp + (((4 * n + qd) ^ c16) << 4)) = acc2[n][s];
        }
    } else {
        const int fu_off = WS_FU + (x * 32 + (lane & 31)) * 128;
        const u16* w2L = w2 + (size_t)(224 * w) * D + 64 * ct;
        const unsigned xs0 = (unsigned)((rp * 128 + 32 * j + (lane >> 3)) * D + (((lane & 7) ^ qd) * 8)) * 2u;
        const unsigned xs1 = (unsigned)((rp * 128 + 32 * j + (lane >> 3)) * D + (((lane & 7) ^ (4 + qd)) * 8)) * 2u + 7168u;
        const unsigned ws0 = (unsigned)((16 * j + qd) * F + fp * 128 + (((((c16 >> 1) ^ qd) << 1) | (lane & 1)) * 8)) * 2u;
        const unsigned ws1 = (unsigned)((16 * j + qd) * F + fp * 128 + (((((c16 >> 1) ^ (qd | 4)) << 1) | (lane & 1)) * 8)) * 2u + 2u * 13312u;
#define STAGE(c)                                                                                   \
    do {                                                                                           \
        LAS char* sl = sm + ((c) & 3) * 32768 + 4096 * j;                                          \
        glds16x4(q + (c) * 64, xs0, xs1, xs0 + 14336u, xs1 + 14336u, sl);                          \
        glds16x4(w1 + (c) * 64 * F, ws0, ws0 + 13312u, ws1, ws1 + 13312u, sl + 16384);             \
    } while (0)
        const char* hrow = (const char*)hk + (size_t)(d0 + ch) * 4096;
        const int urow = WS_UT + (d0 + ch) * 4096;
        v4u ha0, ha1, ub0, ub1; v2u hb0, hb1, ua0, ua1;
#define HKLOAD() asm volatile("global_load_dwordx4 %0, %4, %6\n\tglobal_load_dwordx2 %1, %4, %6 offset:16\n\t" \
                     "global_load_dwordx4 %2, %4, %6 offset:1024\n\tglobal_load_dwordx2 %3, %5, %6" \
                     : "=&v"(ha0), "=&v"(hb0), "=&v"(ha1), "=&v"(hb1) \
                     : "v"(16 * L0), "v"(L1 == 255 ? 0 : 16 * L1 + 16), "s"(hrow) : "memory")
        if (isG) {
            STAGE(0); STAGE(1); STAGE(2);
            ST(1);
            WAIT_V(16); BAR(); STAGE(3);
            ST(2);
            WAIT_V(16); BAR(); STAGE(4);
            WAIT_V(16); BAR(); STAGE(5);
            WAIT_V(16); BAR(); STAGE(6);
            ST(3);
            WAIT_V(16); BAR(); STAGE(7);
        } else {
            BAR(); BAR();
            __builtin_amdgcn_s_sleep(30);
        }
        HKLOAD();
        tag = __builtin_amdgcn_readfirstlane(*(volatile LAS unsigned*)(sm + L_FL + 4 * F_TAG));
        if (isG) { WAIT_V(20); BAR(); }
        unsigned pr = 0;
        if (j == 0) asm volatile("global_load_dword %0, %1, %2 sc1" : "=v"(pr) : "v"(fu_off), "s"(ws) : "memory");
        W2STAGE(w2L, 0, PLACE(0)); W2STAGE(w2L, 1, PLACE(1));
        if (isG) { WAIT_V(20); BAR(); }
        W2STAGE(w2L, 2, PLACE(2)); W2STAGE(w2L, 3, PLACE(3));
        asm volatile("s_waitcnt vmcnt(16)" : "+v"(pr), "+v"(ha0), "+v"(hb0), "+v"(ha1), "+v"(hb1) :: "memory");
        const bool utok = j != 0 || __all(pr == tag);
        if (j == 0 && utok) lds_set(sm, F_UT, lane);
        if (isG) { WAIT_L0(); BAR(); }
        ST(4);
        if (!utok) { poll_flags(rs, fu_off, tag); lds_set(sm, F_UT, lane); }
        lds_wait(sm, F_UT, 0, lane);
        asm volatile("global_load_dwordx2 %0, %4, %6 sc1\n\tglobal_load_dwordx4 %1, %5, %6 sc1\n\t"
                     "global_load_dwordx2 %2, %5, %6 offset:1016 sc1\n\tglobal_load_dwordx4 %3, %5, %6 offset:1024 sc1"
                     : "=&v"(ua0), "=&v"(ub0), "=&v"(ua1), "=&v"(ub1)
                     : "v"(urow + (L0 == 0 ? 0 : 16 * L0 - 8)), "v"(urow + 16 * L0), "s"(ws) : "memory");
        W2STAGE(w2L, 4, PLACE(4)); W2STAGE(w2L, 5, PLACE(5));
        s16x8 wr[6][4];
        WAIT_V(20);
        W2READ(wr[0], PLACE(0)); W2READ(wr[1], PLACE(1));
        fill_pads_h(sm, ch, half, lane);
        fill_h(sm, ha0, hb0, ch, L0); fill_h(sm, ha1, hb1, ch, L1);
        WAIT_L0(); SCHED();
        ST(5);
        W2STAGE(w2C, 0, PLACE(0)); W2STAGE(w2C, 1, PLACE(1));
        if (isG) lds_wait(sm, F_C7, 3, lane);
        asm volatile("s_waitcnt vmcnt(16)" : "+v"(ua0), "+v"(ub0), "+v"(ua1), "+v"(ub1) :: "memory");
        W2READ(wr[2], PLACE(2)); W2READ(wr[3], PLACE(3));
        fill_pads_u(sm, ch, half, lane);
        fill_u(sm, ua0, ub0, ch, L0); fill_u(sm, ua1, ub1, ch, L1);
        WAIT_L0(); SCHED();
        lds_set(sm, F_FILL + j, lane);
        ST(6);
        W2STAGE(w2C, 2, PLACE(2)); W2STAGE(w2C, 3, PLACE(3));
        WAIT_V(16);
        W2READ(wr[4], PLACE(4)); W2READ(wr[5], PLACE(5));
        WAIT_L0(); SCHED();
        W2STAGE(w2L, 6, PLACE(4)); W2STAGE(w2C, 4, PLACE(5));
        WAIT_V(16);
        lds_set(sm, F_C01 + j, lane);
        lds_wait(sm, F_PART + j, 0, lane);
        W2STAGE(w2C, 5, PLACE(0)); W2STAGE(w2C, 6, PLACE(1));
        ST(7);
        {
            const int kpl = ((7 * (w - 4 * ((lane >> 2) & 1))) >> 1) + (lane & 3);
            poll_flags(rs, WS_FH + ((rt >> 1) * 16 + (kpl >> 1)) * 128 + ((rt & 1) + 2 * (kpl & 1)) * 4, tag);
        }
        ST(11);
        v4u hbuf[7][4];
        f32x4 acc2[4][4] = {};
        lds_set(sm, F_W2OK + j, lane);
        HLOAD(0); HLOAD(1); HLOAD(2); HLOAD(3);
        G2MMA(wr[0], 0); HLOAD(4); HLOAD(5);
        G2MMA(wr[1], 1); HLOAD(6);
        qv = *(const GAS v4u*)(q + (size_t)(64 * rt + es) * D + 64 * ct + ed);
        SCHED();
        s16x8 wl[4];
        G2MMA(wr[2], 2);
        G2MMA(wr[3], 3);
        G2MMA(wr[4], 4);
        W2READ(wl, PLACE(4)); SCHED(); G2MMA(wr[5], 5);
        G2MMA(wl, 6);
        ST(12);
        const int zoff = WS_Z + (64 * ct + 16 * j + qd) * 8192 + rt * 256 + c16 * 16;
        v4u z[4]; v2u gr;
#pragma unroll
        for (int i = 0; i < 4; ++i) z[i] = ld128(rs, zoff + i * 4 * 8192);
        gr = ld64(rs, WS_G + (64 * rt + lane) * 8);
        SCHED();
        lds_wait(sm, F_CONV, 3, lane);
#pragma unroll
        for (int s = 0; s < 4; ++s) {
            LAS char* pp = sm + (s < 3 ? L_CV + j * 16384 + s * 4096 : PLACE(4)) + c16 * 256;
#pragma unroll
            for (int n = 0; n < 4; ++n) *(LAS f32x4*)(pp + (((4 * n + qd) ^ c16) << 4)) = acc2[n][s];
        }
        SCHED();
#pragma unroll 1
        for (unsigned spins = 0; spins < SPIN_CAP; ++spins) {
            bool ok = gr.y == tag;
#pragma unroll
            for (int i = 0; i < 4; ++i) ok = ok && z[i].y == tag && z[i].w == tag;
            if (__all(ok)) break;
            __builtin_amdgcn_s_sleep(2);
            CBAR();
#pragma unroll
            for (int i = 0; i < 4; ++i) z[i] = ld128(rs, zoff + i * 4 * 8192);
            gr = ld64(rs, WS_G + (64 * rt + lane) * 8);
        }
#pragma unroll
        for (int i = 0; i < 4; ++i) *(LAS v2u*)(sm + L_CV + j * 16384 + 12288 + (4 * i + qd) * 128 + c16 * 8) = (v2u){z[i].x, z[i].z};
        if (j == 0) *(LAS unsigned*)(sm + L_CV + 12288 + 2048 + lane * 4) = gr.x;
        if (b == 255 && w == 5) {
#pragma unroll 1
            for (unsigned spins = 0; spins < SPIN_CAP; ++spins) {
                CBAR();
                bool ok = true;
#pragma unroll
                for (int i = 0; i < 4; ++i) ok = (ld32(rs, WS_FU + (i * 64 + lane) * 128) == tag) && ok;
                if (__all(ok)) break;
                __builtin_amdgcn_s_sleep(2);
            }
            if (lane == 0) st32(tag, rs, WS_E);
        }
    }
    WAIT_L0();
    ST(13);
    BAR();
    {
        const int zr = ed + qq;
        LAS char* zp = sm + L_CV + 12288 + (zr >> 4) * 16384 + (zr & 15) * 128 + 32 * j + 8 * p4;
        v2u za = __builtin_bit_cast(v2u, trread(zp)), zb = __builtin_bit_cast(v2u, trread(zp + 512));
        const float gg = *(LAS float*)(sm + L_CV + 12288 + 2048 + es * 4);
        const int sl0 = ((ed >> 2) ^ c16) << 4, sl1 = (((ed >> 2) + 1) ^ c16) << 4;
        LAS char* pp = sm + (j >> 1) * 32768 + (j & 1) * 4096 + c16 * 256;
        LAS char* pl = sm + (j < 3 ? L_CV + j * 4096 : 65536) + c16 * 256;
        const int lstr = j < 3 ? 16384 : 8192;
        f32x4 s0 = *(LAS f32x4*)(pp + sl0), s1 = *(LAS f32x4*)(pp + sl1);
#pragma unroll
        for (int i = 1; i < 4; ++i) { s0 += *(LAS f32x4*)(pp + i * 8192 + sl0); s1 += *(LAS f32x4*)(pp + i * 8192 + sl1); }
#pragma unroll
        for (int i = 0; i < 4; ++i) { s0 += *(LAS f32x4*)(pl + i * lstr + sl0); s1 += *(LAS f32x4*)(pl + i * lstr + sl1); }
        const float g1 = rbf(1.0f - gg);
        unsigned zw[4] = {za.x, za.y, zb.x, zb.y};
        float bs[8] = {s0[0], s0[1], s0[2], s0[3], s1[0], s1[1], s1[2], s1[3]};
        v4u o;
#pragma unroll
        for (int e = 0; e < 4; ++e) {
            unsigned bb = pk2(bs[2 * e], bs[2 * e + 1]);
            unsigned aa = pk2(bflo(qv[e]) * bflo(zw[e]), bfhi(qv[e]) * bfhi(zw[e]));
            unsigned ga = pk2(gg * bflo(aa), gg * bfhi(aa));
            unsigned gb = pk2(g1 * bflo(bb), g1 * bfhi(bb));
            o[e] = pk2(bflo(ga) + bflo(gb), bfhi(ga) + bfhi(gb));
        }
        *(GAS v4u*)(out + (size_t)(64 * rt + es) * D + 64 * ct + ed) = o;
    }
    asm volatile(".Lfused_end:" ::: "memory");
    ST(14);
    ST(15);
    ST_END(isG ? 0 : 1);
}

extern "C" void kernel_launch(void* const* d_in, const int* in_sizes, int n_in, void* d_out, int out_size, void* d_ws, size_t ws_size,
                              hipStream_t stream) {
    const u16* q = (const u16*)d_in[0]; const u16* k = (const u16*)d_in[1]; const u16* v = (const u16*)d_in[2];
    const u16* sk = (const u16*)d_in[3]; const u16* hk = (const u16*)d_in[4]; const u16* Dv = (const u16*)d_in[5];
    const u16* w1 = (const u16*)d_in[7]; const u16* w2 = (const u16*)d_in[8]; const u16* gw = (const u16*)d_in[9];
    fused<<<256, 512, 0, stream>>>(q, k, v, sk, hk, Dv, w1, w2, gw, (char*)d_ws, (u16*)d_out);
}
```

```cpp
#include <hip/hip_runtime.h>
#include <hip/hip_bf16.h>
#include <stdint.h>

#define LAS __attribute__((address_space(3)))
#define GAS __attribute__((address_space(1)))
typedef unsigned short u16;
typedef short s16x4 __attribute__((ext_vector_type(4)));
typedef short s16x8 __attribute__((ext_vector_type(8)));
typedef float f32x4 __attribute__((ext_vector_type(4)));
typedef unsigned v4u __attribute__((ext_vector_type(4)));
typedef unsigned v2u __attribute__((ext_vector_type(2)));
typedef __bf16 bf16x2_t __attribute__((ext_vector_type(2)));
typedef float f32x2 __attribute__((ext_vector_type(2)));
typedef __amdgpu_buffer_rsrc_t rsrc_t;

constexpr int S = 2048, D = 512, F = 1792;
#define PFA 3
#define PFB 4
#define HK2V 24
#define LPRIO 2
#define HKV 14

constexpr int WS_HF = 0;
constexpr int WS_UT = 8 << 20;
constexpr int WS_Z = 10 << 20;
constexpr int WS_G = 14 << 20;
constexpr int WS_FU = WS_G + 65536;
constexpr int WS_FH = WS_FU + 32768;
constexpr int WS_E = WS_FH + 32768;
constexpr int WS_BYTES = 16 << 20;
constexpr int SC1 = 16;
constexpr unsigned SPIN_CAP = 1u << 16;

__device__ __forceinline__ float bflo(unsigned w) { return __uint_as_float(w << 16); }
__device__ __forceinline__ float bfhi(unsigned w) { return __uint_as_float(w & 0xFFFF0000u); }
__device__ __forceinline__ unsigned pk2v(f32x2 f) { return __builtin_bit_cast(unsigned, __builtin_convertvector(f, bf16x2_t)); }
__device__ __forceinline__ unsigned pk2(float lo, float hi) { return pk2v((f32x2){lo, hi}); }
__device__ __forceinline__ float rbf(float f) { return bflo(pk2(f, f)); }
__device__ __forceinline__ s16x4 trread(LAS char* p) { return __builtin_amdgcn_ds_read_tr16_b64_v4i16((LAS s16x4*)p); }
__device__ __forceinline__ s16x8 cat8(s16x4 a, s16x4 b) { return __builtin_shufflevector(a, b, 0, 1, 2, 3, 4, 5, 6, 7); }
__device__ __forceinline__ void glds16(const void* base, unsigned voff, LAS char* l) {
    asm volatile("s_mov_b32 m0, %2\n\ts_nop 0\n\tglobal_load_lds_dwordx4 %0, %1"
                 :: "v"(voff), "s"(base), "s"((unsigned)(size_t)l) : "memory");
}
__device__ __forceinline__ void glds16x4(const void* base, unsigned vo0, unsigned vo1, unsigned vo2, unsigned vo3, LAS char* l) {
    asm volatile("s_mov_b32 m0, %5\n\ts_nop 0\n\tglobal_load_lds_dwordx4 %0, %4\n\tglobal_load_lds_dwordx4 %1, %4 offset:1024\n\t"
                 "global_load_lds_dwordx4 %2, %4 offset:2048\n\tglobal_load_lds_dwordx4 %3, %4 offset:3072"
                 :: "v"(vo0), "v"(vo1), "v"(vo2), "v"(vo3), "s"(base), "s"((unsigned)(size_t)l) : "memory");
}
#define WAIT_V(n) asm volatile("s_waitcnt vmcnt(" #n ")" ::: "memory")
#define WAIT_V0C() __builtin_amdgcn_s_waitcnt(0x0F70)
#define WAIT_L0() __builtin_amdgcn_s_waitcnt(0xC07F)
#define BAR() asm volatile("s_barrier" ::: "memory")
#define SCHED() __builtin_amdgcn_sched_barrier(0)
#define CBAR() asm volatile("" ::: "memory")
#define ST_DECL
#define ST(i)
#define ST_END(k)
#define STF(i)
#define MFMA(a, b, c) __builtin_amdgcn_mfma_f32_16x16x32_bf16(a, b, c, 0, 0, 0)

__device__ __forceinline__ v4u ld128(rsrc_t rs, int off) { return __builtin_amdgcn_raw_buffer_load_b128(rs, off, 0, SC1); }
__device__ __forceinline__ v2u ld64(rsrc_t rs, int off) { return __builtin_amdgcn_raw_buffer_load_b64(rs, off, 0, SC1); }
__device__ __forceinline__ unsigned ld32(rsrc_t rs, int off) { return __builtin_amdgcn_raw_buffer_load_b32(rs, off, 0, SC1); }
__device__ __forceinline__ void st128(v4u v, rsrc_t rs, int off) { __builtin_amdgcn_raw_buffer_store_b128(v, rs, off, 0, SC1); }
__device__ __forceinline__ void st64(v2u v, rsrc_t rs, int off) { __builtin_amdgcn_raw_buffer_store_b64(v, rs, off, 0, SC1); }
__device__ __forceinline__ void st32(unsigned v, rsrc_t rs, int off) { __builtin_amdgcn_raw_buffer_store_b32(v, rs, off, 0, SC1); }
__device__ __forceinline__ void poll_flags(rsrc_t rs, int off, unsigned tag) {
#pragma unroll 1
    for (unsigned spins = 0; spins < SPIN_CAP; ++spins) {
        CBAR();
        unsigned v = ld32(rs, off);
        if (__all(v == tag)) break;
        __builtin_amdgcn_s_sleep(4);
    }
    CBAR();
}
__device__ __forceinline__ v4u silu8(f32x4 a0, f32x4 a1) {
    f32x2 x[4] = {{a0[0], a0[1]}, {a0[2], a0[3]}, {a1[0], a1[1]}, {a1[2], a1[3]}};
    f32x2 t[4];
#pragma unroll
    for (int i = 0; i < 4; ++i) t[i] = x[i] * -1.44269504088896f;
#pragma unroll
    for (int i = 0; i < 4; ++i) { t[i].x = __builtin_amdgcn_exp2f(t[i].x); t[i].y = __builtin_amdgcn_exp2f(t[i].y); }
#pragma unroll
    for (int i = 0; i < 4; ++i) t[i] = t[i] + 1.0f;
#pragma unroll
    for (int i = 0; i < 4; ++i) { t[i].x = __builtin_amdgcn_rcpf(t[i].x); t[i].y = __builtin_amdgcn_rcpf(t[i].y); }
    v4u o;
#pragma unroll
    for (int i = 0; i < 4; ++i) o[i] = pk2v(x[i] * t[i]);
    return o;
}

constexpr int L_CV = 98304;
constexpr int L_KV = 131072;
constexpr int L_T = L_KV + 22528;
constexpr int U_CP = 4416, U_CH = 4 * U_CP, H_CP = 6400, H_CH = 2 * H_CP;
constexpr int L_U = L_CV, L_H = L_CV + 2 * U_CH;
constexpr int LDS_BYTES = 163840;
constexpr int L_ZS = L_H + 2 * H_CH;
constexpr int L_FL = LDS_BYTES - 128;
enum { F_W2OK = 0, F_C7 = 4, F_FILL = 8, F_CONV = 12, F_C01 = 16, F_PART = 20, F_TAG = 24, F_UT = 25 };

__device__ __forceinline__ void lds_set(LAS char* sm, int i, int lane) {
    if (lane == 0) *(volatile LAS unsigned*)(sm + L_FL + 4 * i) = 1u;
}
__device__ __forceinline__ void lds_wait(LAS char* sm, int i0, int mask, int lane) {
#pragma unroll 1
    for (unsigned spins = 0; spins < (1u << 22); ++spins) {
        unsigned v = *(volatile LAS unsigned*)(sm + L_FL + 4 * (i0 + (lane & mask)));
        if (__all(v != 0u)) break;
        __builtin_amdgcn_s_sleep(1);
    }
    CBAR();
}

__device__ __forceinline__ v4u kv_prep(LAS char* kvw, int e, int lane, bool zero_halo) {
    const int gq = lane >> 4, qq = (lane & 15) >> 2, p = lane & 3;
    LAS char* kb = kvw + (8 * e + 4 + qq) * 128 + 32 * gq + 8 * p;
    v2u k0 = __builtin_bit_cast(v2u, trread(kb));
    v2u k1 = __builtin_bit_cast(v2u, trread(kb + 512));
    v2u k2 = __builtin_bit_cast(v2u, trread(kb + 1024));
    LAS char* vb = kvw + 3072 + (8 * e + qq) * 128 + 32 * gq + 8 * p;
    v2u v0 = __builtin_bit_cast(v2u, trread(vb));
    v2u v1 = __builtin_bit_cast(v2u, trread(vb + 512));
    v2u sk = *(LAS v2u*)(kvw + 5120 + 8 * lane);
    if (zero_halo) { k0.x = 0; k0.y = 0; }
    float kk[12] = {bflo(k0.x), bfhi(k0.x), bflo(k0.y), bfhi(k0.y), bflo(k1.x), bfhi(k1.x), bflo(k1.y), bfhi(k1.y),
                    bflo(k2.x), bfhi(k2.x), bflo(k2.y), bfhi(k2.y)};
    float vv[8] = {bflo(v0.x), bfhi(v0.x), bflo(v0.y), bfhi(v0.y), bflo(v1.x), bfhi(v1.x), bflo(v1.y), bfhi(v1.y)};
    const float w0 = bflo(sk.x), w1 = bfhi(sk.x), w2 = bflo(sk.y), w3 = bfhi(sk.y);
    float ks[8];
#pragma unroll
    for (int i = 0; i < 8; ++i) ks[i] = kk[i + 1] * w3 + kk[i + 2] * w2 + kk[i + 3] * w1 + kk[i + 4] * w0;
    float u[8];
#pragma unroll
    for (int i = 0; i < 8; i += 2) {
        unsigned r = pk2(ks[i], ks[i + 1]);
        u[i] = bflo(r) * vv[i];
        u[i + 1] = bfhi(r) * vv[i + 1];
    }
    return (v4u){pk2(u[7], u[6]), pk2(u[5], u[4]), pk2(u[3], u[2]), pk2(u[1], u[0])};
}

__device__ __forceinline__ unsigned ab16(unsigned hi, unsigned lo) { return __builtin_amdgcn_alignbit(hi, lo, 16); }
__device__ __forceinline__ void fill_u(LAS char* sm, v2u ua, v4u ubv, int ch, int L) {
    if (L == 0) { ua.x = 0; ua.y = 0; }
    const unsigned w0 = ua.x, w1 = ua.y, w2 = ubv.x, w3 = ubv.y, w4 = ubv.z, w5 = ubv.w;
    const unsigned t0 = ab16(w1, w0), t1 = ab16(w2, w1), t2 = ab16(w3, w2), t3 = ab16(w4, w3), t4 = ab16(w5, w4);
    LAS char* ub = sm + L_U + ch * U_CH;
    LAS char* up = ub + 16 * L + 64;
    *(LAS v4u*)(up) = (v4u){t1, t2, t3, t4};
    *(LAS v4u*)(up + U_CP) = (v4u){w1, w2, w3, w4};
    *(LAS v4u*)(up + 2 * U_CP) = (v4u){t0, t1, t2, t3};
    *(LAS v4u*)(up + 3 * U_CP) = (v4u){w0, w1, w2, w3};
    if (L == 255) {
        LAS char* e = ub + 16 * 256 + 64;
        *(LAS v4u*)(e) = (v4u){w5 >> 16, 0, 0, 0};
        *(LAS v4u*)(e + U_CP) = (v4u){w5, 0, 0, 0};
        *(LAS v4u*)(e + 2 * U_CP) = (v4u){t4, w5 >> 16, 0, 0};
        *(LAS v4u*)(e + 3 * U_CP) = (v4u){w4, w5, 0, 0};
    }
}
__device__ __forceinline__ void fill_h(LAS char* sm, v4u ha, v2u hbv, int ch, int L) {
    if (L == 255) { hbv.x = 0; hbv.y = 0; }
    LAS char* hb = sm + L_H + ch * H_CH;
    LAS char* hp = hb + ((L >> 3) + 8) * 160 + (L & 7) * 16;
    *(LAS v4u*)(hp) = ha;
    *(LAS v4u*)(hp + H_CP) = (v4u){ha.z, ha.w, hbv.x, hbv.y};
    if (L == 0) *(LAS v4u*)(hb + H_CP + 7 * 160 + 7 * 16) = (v4u){0, 0, ha.x, ha.y};
}
__device__ __forceinline__ void fill_pads_h(LAS char* sm, int ch, int half, int lane) {
    const v4u z = {0, 0, 0, 0};
    LAS char* hb = sm + L_H + ch * H_CH;
    if (!(half == 1 && lane == 63)) *(LAS v4u*)(hb + half * H_CP + (lane >> 3) * 160 + (lane & 7) * 16) = z;
}
__device__ __forceinline__ void fill_pads_u(LAS char* sm, int ch, int half, int lane) {
    const v4u z = {0, 0, 0, 0};
    if (lane < 16) {
        LAS char* ub = sm + L_U + ch * U_CH;
        const int c = 2 * half + (lane >> 3), jx = lane & 7;
        if (jx != 4) *(LAS v4u*)(ub + c * U_CP + (jx < 4 ? 16 * jx : 16 * (252 + jx) + 64)) = z;
    }
}

constexpr int conv_want(int t, int T0, int NA) {
    int wnt = (t + PFA < T0) ? t + PFA : (t + PFB > T0 - 1 ? t + PFB : T0 - 1);
    return t < -1 ? -1 : (wnt < NA - 1 ? wnt : NA - 1);
}
template <int I> struct IC { static constexpr int v = I; };
template <int A, int B, class Fn> __device__ __forceinline__ void static_for(Fn&& fn) {
    if constexpr (A < B) { fn(IC<A>{}); static_for<A + 1, B>(fn); }
}
template <int NTA, int NTB, class Hook>
__device__ __forceinline__ void conv_body(LAS char* ua, LAS char* hb, f32x4& aA0, f32x4& aA1, f32x4& aB0, f32x4& aB1, Hook hook) {
    constexpr int NA = 16 * NTA + 17, NB = 16 * NTB + 17, T0 = NA - NB;
    constexpr int HK = HKV, HK2 = HK2V;
    LAS char* up = ua - 64 * (NA - 1);
    asm volatile("" : "+v"(up), "+v"(hb));
    s16x8 W[NA + 1], BA[NA], BB[NB];
#define HOFF(t) (((t) & 1) ? (((t) + 1) / 2) * 160 : ((t) / 2) * 160 + 64)
    W[0] = *(LAS s16x8*)up;
    constexpr int PF = PFA;
    static_for<0, PF>([&](auto nc) __attribute__((always_inline)) {
        constexpr int nl = decltype(nc)::v;
        W[nl + 1] = *(LAS s16x8*)(up + 64 * (nl + 1));
        BA[nl] = *(LAS s16x8*)(hb + HOFF(nl));
    });
    SCHED();
    static_for<0, NA>([&](auto tc) __attribute__((always_inline)) {
        constexpr int t = decltype(tc)::v, nl = t + PF;
        aA1 = MFMA(W[t], BA[t], aA1);
        SCHED();
        if constexpr (nl + 1 < NA) W[nl + 1] = *(LAS s16x8*)(up + 64 * (nl + 1));
        SCHED();
        if constexpr (t + 1 < NA) aA0 = MFMA(W[t + 1], BA[t], aA0);
        SCHED();
        if constexpr (nl < NA) BA[nl] = *(LAS s16x8*)(hb + HOFF(nl));
        SCHED();
        if constexpr (t >= T0) { aB1 = MFMA(W[t], BB[t - T0], aB1); SCHED(); }
        if constexpr (nl >= T0 && nl < NA) { BB[nl - T0] = *(LAS s16x8*)(hb + HOFF(nl - T0)); SCHED(); }
        if constexpr (t >= T0 && t + 1 < NA) { aB0 = MFMA(W[t + 1], BB[t - T0], aB0); SCHED(); }
        if constexpr (t == HK) { hook(-1); SCHED(); }
    });
}

__global__ __launch_bounds__(512, 2) void fused(const u16* __restrict__ q, const u16* __restrict__ kin, const u16* __restrict__ vin,
                                                const u16* __restrict__ sk, const u16* __restrict__ hk, const u16* __restrict__ Dv,
                                                const u16* __restrict__ w1, const u16* __restrict__ w2, const u16* __restrict__ gw,
                                                char* __restrict__ ws, u16* __restrict__ out) {
    __shared__ __attribute__((aligned(16))) char smem[LDS_BYTES];
    LAS char* sm = (LAS char*)smem;
    const int b = blockIdx.x, x = b & 7, idx = b >> 3;
    const int tid = threadIdx.x, lane = tid & 63;
    const int w = __builtin_amdgcn_readfirstlane(tid >> 6);
    const int j = w & 3;
    const rsrc_t rs = __builtin_amdgcn_make_buffer_rsrc(ws, 0, WS_BYTES, 0x00020000);
    ST_DECL; ST(0);
    unsigned long long pc; unsigned len;
    asm volatile(".Lfused_pc:\n\ts_getpc_b64 %0\n\ts_mov_b32 %1, .Lfused_end-.Lfused_pc" : "=s"(pc), "=s"(len));
    unsigned ctch;
    {
        unsigned co = (unsigned)(idx * 8 + w) * 128u;
        co = co < len ? co : 0u;
        asm volatile("global_load_dword %0, %1, %2" : "=v"(ctch) : "v"(co), "s"(pc) : "memory");
    }
    asm volatile("" :: "s"(q), "s"(kin), "s"(vin), "s"(sk), "s"(hk), "s"(Dv), "s"(w1), "s"(w2), "s"(gw), "s"(ws), "s"(out));
    const int rt = 4 * x + (idx & 3), ct = idx >> 2;
    const int d0 = 64 * x + 2 * idx;
    const int c16 = lane & 15, qd = lane >> 4, qq = c16 >> 2, p4 = lane & 3;
    const bool isG = idx < 28;
    const int rp = 4 * (x >> 1) + (idx & 3), fp = 7 * (x & 1) + (idx >> 2);
    const int ch = j >> 1, half = j & 1;
    const int L0 = 128 * half + lane, L1 = L0 + 64;

#define PLACE(i) (((i) >> 1) * 32768 + j * 8192 + ((i) & 1) * 4096)
    const int w2rd = (4 * qd + qq) * 128 + 32 * ((2 * qd + (qq >> 1)) & 3) + 8 * p4;
#define W2READ(dst, addr)                                                                    \
    do {                                                                                     \
        _Pragma("unroll") for (int n_ = 0; n_ < 4; ++n_) {                                   \
            LAS char* a_ = sm + (addr) + (w2rd ^ (n_ << 5));                                 \
            dst[n_] = cat8(trread(a_), trread(a_ + 2048));                                   \
        }                                                                                    \
    } while (0)
    const unsigned w2off = (unsigned)((lane >> 3) * D + ((((((lane & 7) >> 1) ^ qd) << 1) | (lane & 1)) * 8)) * 2u;
    const u16* w2C = w2 + (size_t)(224 * j) * D + 64 * ct;
#define W2STAGE(base, k_, addr) glds16x4(base + (32 * (k_)) * D, w2off, w2off + 7168u, w2off + 14336u, w2off + 21504u, sm + (addr))
    const int h_off = WS_HF + (rt * 56 + 7 * w) * 4096 + lane * 16;
    const int es = 16 * j + c16, ed = 32 * (w >> 2) + 8 * qd;
    unsigned tag;
    v4u qv;
#define HLOAD(k_) do { _Pragma("unroll") for (int i_ = 0; i_ < 4; ++i_) hbuf[k_][i_] = ld128(rs, h_off + (k_) * 4096 + i_ * 1024); SCHED(); } while (0)
#define G2MMA(wf, k_) do { _Pragma("unroll") for (int n_ = 0; n_ < 4; ++n_) _Pragma("unroll") for (int s_ = 0; s_ < 4; ++s_) \
        acc2[n_][s_] = MFMA(wf[n_], __builtin_bit_cast(s16x8, hbuf[k_][s_]), acc2[n_][s_]); SCHED(); } while (0)
#define LPIECE(i) ((i) == 15 ? PLACE(4) + 2048 : L_CV + j * 16384 + (i) * 1024)

    unsigned ev = 0, dvr = 0, tch = 0;
    LAS char* kvw = sm + L_KV + j * 5632;
    if (w < 4) {
        const int r_ = 64 * idx + 16 * j - 8 + (lane >> 3);
        const unsigned co_ = (unsigned)(64 * x + (lane & 7) * 8) * 2u;
        glds16(kin, (unsigned)((r_ < 0 ? 0 : r_) * D) * 2u + co_, kvw);
        const unsigned o1_ = (unsigned)((r_ + 8) * D) * 2u + co_;
        glds16(kin, o1_, kvw + 1024);
        glds16(vin, o1_, kvw + 3072);
        if (lane < 32) glds16(sk, (unsigned)(256 * x + lane * 8) * 2u, kvw + 5120);
        glds16(kin + 8 * D, o1_, kvw + 2048);
        glds16(vin + 8 * D, o1_, kvw + 4096);
        asm volatile("global_load_dword %0, %2, %3 sc1\n\tglobal_load_ushort %1, %2, %4" : "=&v"(ev), "=&v"(dvr) : "v"(0), "s"(ws + WS_E), "s"(Dv + d0 + ch) : "memory");
        const int fpc = fp < 14 ? fp : 13, l5 = (lane & 31) + 32 * (j & 1);
        const int id = 128 + fpc * 64 + l5;
        const unsigned to = j < 2 ? (unsigned)((rp * 128 + (id & 127)) * 1024 + (id >> 7) * 128) : (unsigned)((32 * rp + (l5 >> 1)) * (F * 2) + fpc * 256 + (l5 & 1) * 128);
        asm volatile("global_load_dword %0, %1, %2" : "=v"(tch) : "v"(to), "s"(j < 2 ? (const char*)q : (const char*)w1) : "memory");
    }
    SCHED();

    if (w < 4) {
        unsigned dvs;
        if (w == 0 && lane < 32 && lane != F_TAG) *(LAS unsigned*)(sm + L_FL + 4 * lane) = 0u;
        const int sh = j & 1, fh = j >> 1;
        LAS char* bad[2][2]; LAS char* aad[2][4];
        {
            const int key = qq | ((qd & 1) << 2);
#pragma unroll
            for (int sp = 0; sp < 2; ++sp) {
                bad[sp][0] = sm + sp * 65536 + (64 * sh + c16) * 128 + 16 * (qd ^ (c16 >> 1));
                bad[sp][1] = sm + sp * 65536 + (64 * sh + c16) * 128 + 16 * ((4 + qd) ^ (c16 >> 1));
#pragma unroll
                for (int ft = 0; ft < 4; ++ft) aad[sp][ft] = sm + sp * 65536 + 16384 + (8 * qd + qq) * 256 + 32 * ((4 * fh + ft) ^ key) + 8 * p4;
                asm volatile("" : "+v"(bad[sp][0]), "+v"(bad[sp][1]), "+v"(aad[sp][0]), "+v"(aad[sp][1]), "+v"(aad[sp][2]), "+v"(aad[sp][3]));
            }
        }
        LAS char* tsrc = sm + L_T + (16 * j + (lane >> 3)) * 144 + (lane & 7) * 16;
        int tdst = WS_UT + (64 * x + 16 * j + (lane >> 3)) * 4096 + (1984 - 64 * idx) * 2 + (lane & 7) * 16;
        asm volatile("" : "+v"(tsrc), "+v"(tdst));
        WAIT_V(5);
        ST(1);
        *(LAS v4u*)(sm + L_T + lane * 144 + (7 - 2 * j) * 16) = kv_prep(kvw, 0, lane, idx == 0 && j == 0);
        SCHED(); WAIT_V(3);
        *(LAS v4u*)(sm + L_T + lane * 144 + (6 - 2 * j) * 16) = kv_prep(kvw, 1, lane, false);
        WAIT_L0(); SCHED();
        ST(2);
        BAR();
#define UT_STORE() do { v4u o0_ = *(LAS v4u*)tsrc, o1_ = *(LAS v4u*)(tsrc + 8 * 144); st128(o0_, rs, tdst); st128(o1_, rs, tdst + 8 * 4096); } while (0)
#define GET_TAG() do { \
            asm volatile("s_waitcnt vmcnt(0)\n\tv_readfirstlane_b32 %0, %2\n\tv_readfirstlane_b32 %1, %3\n\ts_nop 3" : "=s"(tag), "=s"(dvs) : "v"(ev), "v"(dvr), "v"(tch), "v"(ctch) : "memory"); \
            tag += 1u; \
            if (tid == 0) *(LAS unsigned*)(sm + L_FL + 4 * F_TAG) = tag; } while (0)
        const int fu_st = WS_FU + (x * 32 + idx) * 128;
        if (isG) {
            f32x4 acc[4][4] = {};
            s16x8 fa[2][2][4], fb[2][2][4];
#define READ(c)                                                                                     \
    do {                                                                                            \
        _Pragma("unroll") for (int kk = 0; kk < 2; ++kk) {                                          \
            _Pragma("unroll") for (int ft = 0; ft < 4; ++ft) {                                      \
                fb[(c) & 1][kk][ft] = *(LAS s16x8*)(bad[((c) >> 1) & 1][kk] + ((c) & 1) * 32768 + 2048 * ft); \
                LAS char* a_ = aad[((c) >> 1) & 1][ft] + ((c) & 1) * 32768 + kk * 8192;             \
                fa[(c) & 1][kk][ft] = cat8(trread(a_), trread(a_ + 1024));                          \
            }                                                                                       \
        }                                                                                           \
    } while (0)
#define MMA(c)                                                                                      \
    do {                                                                                            \
        _Pragma("unroll") for (int kk = 0; kk < 2; ++kk)                                            \
            _Pragma("unroll") for (int ft = 0; ft < 4; ++ft)                                        \
                _Pragma("unroll") for (int st = 0; st < 4; ++st)                                    \
                    acc[ft][st] = MFMA(fa[(c) & 1][kk][ft], fb[(c) & 1][kk][st], acc[ft][st]);      \
    } while (0)
#define ILV() do { _Pragma("unroll") for (int g_ = 0; g_ < 24; ++g_) { __builtin_amdgcn_sched_group_barrier(0x100, 1, 0); __builtin_amdgcn_sched_group_barrier(0x8, 1, 0); } \
                   __builtin_amdgcn_sched_group_barrier(0x8, 8, 0); SCHED(); } while (0)
            STF(1); SCHED(); UT_STORE(); READ(0); SCHED(); WAIT_L0(); STF(2);
            BAR(); STF(3); SCHED(); READ(1); MMA(0); ILV(); WAIT_L0(); STF(4);
            BAR(); STF(5); SCHED(); READ(2); MMA(1); ILV(); STF(6);
            GET_TAG(); WAIT_L0(); STF(7);
            BAR(); STF(8);
            if (tid == 0) st32(tag, rs, fu_st);
            SCHED(); READ(3); MMA(2); ILV(); WAIT_L0(); STF(9);
            ST(3);
            BAR(); STF(10); SCHED(); READ(4); MMA(3); ILV(); WAIT_L0(); STF(11);
            BAR(); STF(12); SCHED(); READ(5); MMA(4); ILV(); WAIT_L0(); STF(13);
            BAR(); STF(14); SCHED(); READ(6); MMA(5); ILV(); WAIT_L0();
            BAR(); STF(15); SCHED();
            ST(4);
            READ(7); MMA(6); ILV(); WAIT_L0();
            lds_set(sm, F_C7 + j, lane);
            SCHED();
            MMA(7);
            ST(5);
            const int hst = WS_HF + ((2 * rp + sh) * 56 + 4 * fp + 2 * fh) * 4096 + lane * 16;
#pragma unroll
            for (int pp = 0; pp < 2; ++pp)
#pragma unroll
                for (int st = 0; st < 4; ++st) st128(silu8(acc[2 * pp][st], acc[2 * pp + 1][st]), rs, hst + pp * 4096 + st * 1024);
            ST(6);
        } else {
            UT_STORE();
            const int r0 = 512 * (x >> 1) + 64 * (4 * (x & 1) + (idx - 28)) + 16 * j;
            v4u gv = *(const GAS v4u*)(gw + lane * 8);
            float gs[2];
#pragma unroll 1
            for (int hh = 0; hh < 2; ++hh) {
                v4u qr[8];
#pragma unroll
                for (int i = 0; i < 8; ++i) qr[i] = *(const GAS v4u*)(q + (size_t)(r0 + 8 * hh + i) * D + lane * 8);
                if (hh == 0) {
                    GET_TAG(); WAIT_L0();
                    BAR();
                    if (tid == 0) st32(tag, rs, fu_st);
                }
                float s[8];
#pragma unroll
                for (int i = 0; i < 8; ++i) {
                    s[i] = 0.f;
#pragma unroll
                    for (int e = 0; e < 4; ++e) {
                        unsigned r = pk2(bflo(qr[i][e]) * bflo(gv[e]), bfhi(qr[i][e]) * bfhi(gv[e]));
                        s[i] += bflo(r) + bfhi(r);
                    }
                }
#pragma unroll
                for (int i = 0; i < 4; ++i) { const bool hi = lane & 32; float snd = hi ? s[i] : s[i + 4], kp = hi ? s[i + 4] : s[i]; s[i] = kp + __shfl_xor(snd, 32); }
#pragma unroll
                for (int i = 0; i < 2; ++i) { const bool hi = lane & 16; float snd = hi ? s[i] : s[i + 2], kp = hi ? s[i + 2] : s[i]; s[i] = kp + __shfl_xor(snd, 16); }
                { const bool hi = lane & 8; float snd = hi ? s[0] : s[1], kp = hi ? s[1] : s[0]; s[0] = kp + __shfl_xor(snd, 8); }
                s[0] += __shfl_xor(s[0], 4); s[0] += __shfl_xor(s[0], 2); s[0] += __shfl_xor(s[0], 1);
                if (hh == 0) gs[0] = s[0]; else gs[1] = s[0];
            }
            if ((lane & 7) < 2) {
                float t = rbf((lane & 1) ? gs[1] : gs[0]);
                float g = rbf(__builtin_amdgcn_rcpf(1.0f + __builtin_amdgcn_exp2f(-1.44269504088896f * t)));
                st64((v2u){__float_as_uint(g), tag}, rs, WS_G + (r0 + 8 * (lane & 1) + (lane >> 3)) * 8);
            }
        }
#define H_PUBLISH() do { WAIT_V(0); if (isG && lane == 0) st32(tag, rs, WS_FH + (rp * 16 + fp) * 128 + j * 4); ST(8); } while (0)
        s16x8 wl[7][4];
        lds_wait(sm, F_C01 + j, 0, lane);
        W2READ(wl[0], PLACE(0)); W2READ(wl[1], PLACE(1));
        WAIT_L0(); SCHED();
        lds_set(sm, F_PART + j, lane);
        H_PUBLISH();
        lds_wait(sm, F_FILL + 2 * ch, 1, lane);
        CBAR();
        ST(7);
        f32x4 aA0 = {}, aA1 = {}, aB0 = {}, aB1 = {};
        const int b0 = c16 >> 3, b1 = c16 & 7;
        v2u uu[2][2];
        {
            LAS char* u3 = sm + L_U + ch * U_CH + 3 * U_CP + 64 + 2 * (2048 - (8 * qd + 4 * b0 + 64 * b1));
#pragma unroll
            for (int t = 0; t < 2; ++t)
#pragma unroll
                for (int mt = 0; mt < 2; ++mt) uu[t][mt] = *(LAS v2u*)(u3 - 1024 * (t ? half : 3 - half) - 64 * mt);
        }
        {
            LAS char* ua_ = sm + L_U + ch * U_CH + (c16 & 3) * U_CP + 64 + 4096 + 16 * (qd - (c16 >> 2));
            LAS char* hb_ = sm + L_H + ch * H_CH + (c16 >> 3) * H_CP + (c16 & 7) * 160 + qd * 16;
            auto hook = [&](int i) __attribute__((always_inline)) {};
            if (half == 0) conv_body<3, 0>(ua_, hb_, aA0, aA1, aB0, aB1, hook);
            else conv_body<2, 1>(ua_, hb_, aA0, aA1, aB0, aB1, hook);
        }
        ST(9);
        lds_set(sm, F_CONV + j, lane);
        {
            const float dd = bflo(dvs);
            const int srcl = ((lane >> 3) + 8 * (lane & 1) + 16 * ((lane >> 1) & 3)) * 4;
            const int zb = WS_Z + (d0 + ch) * 8192 + (lane >> 3) * 256 + (lane & 7) * 16;
#pragma unroll
            for (int t = 0; t < 2; ++t)
#pragma unroll
                for (int mt = 0; mt < 2; ++mt) {
                    f32x4 y = t ? (mt ? aB1 : aB0) : (mt ? aA1 : aA0);
                    v2u u_ = uu[t][mt];
                    unsigned y01 = pk2(y[0], y[1]), y23 = pk2(y[2], y[3]);
                    unsigned p01 = pk2(bfhi(u_.y) * dd, bflo(u_.y) * dd), p23 = pk2(bfhi(u_.x) * dd, bflo(u_.x) * dd);
                    unsigned z0 = pk2(bflo(y01) + bflo(p01), bfhi(y01) + bfhi(p01));
                    unsigned z1 = pk2(bflo(y23) + bflo(p23), bfhi(y23) + bfhi(p23));
                    z0 = __builtin_amdgcn_ds_bpermute(srcl, z0);
                    z1 = __builtin_amdgcn_ds_bpermute(srcl, z1);
                    st128((v4u){z0, tag, z1, tag}, rs, zb + (t ? half : 3 - half) * 2048 + mt * 128);
                }
            SCHED();
        }
        ST(10);
        lds_wait(sm, F_W2OK + j, 0, lane);
        ST(11);
        v4u hbuf[7][4];
        f32x4 acc2[4][4] = {};
        HLOAD(0); HLOAD(1); HLOAD(2); HLOAD(3);
        G2MMA(wl[0], 0);
        HLOAD(4); HLOAD(5); HLOAD(6);
        qv = *(const GAS v4u*)(q + (size_t)(64 * rt + es) * D + 64 * ct + ed);
        SCHED();
        W2READ(wl[2], PLACE(2)); SCHED(); G2MMA(wl[1], 1);
        W2READ(wl[3], PLACE(3)); SCHED(); G2MMA(wl[2], 2);
        W2READ(wl[4], PLACE(5)); SCHED(); G2MMA(wl[3], 3);
        W2READ(wl[5], PLACE(0)); SCHED(); G2MMA(wl[4], 4);
        W2READ(wl[6], PLACE(1)); SCHED(); G2MMA(wl[5], 5);
        G2MMA(wl[6], 6);
        ST(12);
#pragma unroll
        for (int s = 0; s < 4; ++s) {
            LAS char* pp = sm + PLACE(s) + c16 * 256;
#pragma unroll
            for (int n = 0; n < 4; ++n) *(LAS f32x4*)(pp + (((4 * n + qd) ^ c16) << 4)) = acc2[n][s];
        }
    } else {
        const int fu_off = WS_FU + (x * 32 + (lane & 31)) * 128;
        const u16* w2L = w2 + (size_t)(224 * w) * D + 64 * ct;
        const unsigned xs0 = (unsigned)((rp * 128 + 32 * j + (lane >> 3)) * D + (((lane & 7) ^ qd) * 8)) * 2u;
        const unsigned xs1 = (unsigned)((rp * 128 + 32 * j + (lane >> 3)) * D + (((lane & 7) ^ (4 + qd)) * 8)) * 2u + 7168u;
        const unsigned ws0 = (unsigned)((16 * j + qd) * F + fp * 128 + (((((c16 >> 1) ^ qd) << 1) | (lane & 1)) * 8)) * 2u;
        const unsigned ws1 = (unsigned)((16 * j + qd) * F + fp * 128 + (((((c16 >> 1) ^ (qd | 4)) << 1) | (lane & 1)) * 8)) * 2u + 2u * 13312u;
#define STAGE(c)                                                                                   \
    do {                                                                                           \
        LAS char* sl = sm + ((c) & 3) * 32768 + 4096 * j;                                          \
        glds16x4(q + (c) * 64, xs0, xs1, xs0 + 14336u, xs1 + 14336u, sl);                          \
        glds16x4(w1 + (c) * 64 * F, ws0, ws0 + 13312u, ws1, ws1 + 13312u, sl + 16384);             \
    } while (0)
        const char* hrow = (const char*)hk + (size_t)(d0 + ch) * 4096;
        const int urow = WS_UT + (d0 + ch) * 4096;
        v4u ha0, ha1, ub0, ub1; v2u hb0, hb1, ua0, ua1;
#define HKLOAD() asm volatile("global_load_dwordx4 %0, %4, %6\n\tglobal_load_dwordx2 %1, %4, %6 offset:16\n\t" \
                     "global_load_dwordx4 %2, %4, %6 offset:1024\n\tglobal_load_dwordx2 %3, %5, %6" \
                     : "=&v"(ha0), "=&v"(hb0), "=&v"(ha1), "=&v"(hb1) \
                     : "v"(16 * L0), "v"(L1 == 255 ? 0 : 16 * L1 + 16), "s"(hrow) : "memory")
        if (isG) {
            STAGE(0); STAGE(1); STAGE(2);
            ST(1);
            asm volatile("s_waitcnt vmcnt(16)" :: "v"(ctch) : "memory"); BAR(); STAGE(3);
            ST(2);
            WAIT_V(16); BAR(); STAGE(4);
            WAIT_V(16); BAR(); STAGE(5);
            WAIT_V(16); BAR(); STAGE(6);
            ST(3);
            WAIT_V(16); BAR(); STAGE(7);
        } else {
            unsigned t0_, t1_, t2_, t3_;
            const unsigned wo = (unsigned)((((idx - 28) * 8 + x) * 4 + j) * 112 + lane) * 128u;
            asm volatile("global_load_dword %0, %2, %4 sc1\n\tglobal_load_dword %1, %3, %5 sc1"
                         : "=&v"(t0_), "=&v"(t1_) : "v"((unsigned)lane << 18), "v"((unsigned)(lane & 7) << 18), "s"(ws), "s"(out) : "memory");
            BAR();
            asm volatile("global_load_dword %0, %2, %4\n\tglobal_load_dword %1, %3, %4" : "=&v"(t2_), "=&v"(t3_) : "v"(wo), "v"(wo + (lane < 48 ? 8192u : 0u)), "s"(w2) : "memory");
            BAR();
            asm volatile("s_waitcnt vmcnt(0)" :: "v"(t0_), "v"(t1_), "v"(t2_), "v"(t3_), "v"(ctch) : "memory");
            __builtin_amdgcn_s_sleep(20);
        }
        HKLOAD();
        tag = __builtin_amdgcn_readfirstlane(*(volatile LAS unsigned*)(sm + L_FL + 4 * F_TAG));
        if (isG) { WAIT_V(20); BAR(); }
        unsigned pr = 0;
        if (j == 0) asm volatile("global_load_dword %0, %1, %2 sc1" : "=v"(pr) : "v"(fu_off), "s"(ws) : "memory");
        if (isG) { WAIT_V(12); BAR(); }
        W2STAGE(w2C, 0, PLACE(0)); W2STAGE(w2C, 1, PLACE(1));
        asm volatile("s_waitcnt vmcnt(8)" : "+v"(pr), "+v"(ha0), "+v"(hb0), "+v"(ha1), "+v"(hb1) :: "memory");
        const bool utok = j != 0 || __all(pr == tag);
        if (j == 0 && utok) lds_set(sm, F_UT, lane);
        if (isG) { WAIT_L0(); BAR(); }
        ST(4);
        if (!utok) { poll_flags(rs, fu_off, tag); lds_set(sm, F_UT, lane); }
        lds_wait(sm, F_UT, 0, lane);
        ST(8);
        asm volatile("global_load_dwordx2 %0, %4, %6 sc1\n\tglobal_load_dwordx4 %1, %5, %6 sc1\n\t"
                     "global_load_dwordx2 %2, %5, %6 offset:1016 sc1\n\tglobal_load_dwordx4 %3, %5, %6 offset:1024 sc1"
                     : "=&v"(ua0), "=&v"(ub0), "=&v"(ua1), "=&v"(ub1)
                     : "v"(urow + (L0 == 0 ? 0 : 16 * L0 - 8)), "v"(urow + 16 * L0), "s"(ws) : "memory");
        W2STAGE(w2L, 0, PLACE(2)); W2STAGE(w2L, 1, PLACE(3));
        __builtin_amdgcn_s_setprio(2);
        fill_pads_h(sm, ch, half, lane);
        fill_h(sm, ha0, hb0, ch, L0); fill_h(sm, ha1, hb1, ch, L1);
        SCHED();
        __builtin_amdgcn_s_setprio(0);
        W2STAGE(w2L, 2, PLACE(4)); W2STAGE(w2L, 3, PLACE(5));
        __builtin_amdgcn_s_setprio(2);
        if (isG) lds_wait(sm, F_C7, 3, lane);
        asm volatile("s_waitcnt vmcnt(16)" : "+v"(ua0), "+v"(ub0), "+v"(ua1), "+v"(ub1) :: "memory");
        lds_set(sm, F_C01 + j, lane);
        ST(9);
        fill_pads_u(sm, ch, half, lane);
        fill_u(sm, ua0, ub0, ch, L0); fill_u(sm, ua1, ub1, ch, L1);
        WAIT_L0(); SCHED();
        lds_set(sm, F_FILL + j, lane);
        __builtin_amdgcn_s_setprio(0);
        ST(5);
        s16x8 wr[6][4];
        WAIT_V(8);
        W2READ(wr[0], PLACE(2)); W2READ(wr[1], PLACE(3));
        WAIT_L0(); SCHED();
        W2STAGE(w2L, 4, PLACE(2)); W2STAGE(w2L, 5, PLACE(3));
        WAIT_V(8);
        W2READ(wr[2], PLACE(4)); W2READ(wr[3], PLACE(5));
        WAIT_L0(); SCHED();
        W2STAGE(w2L, 6, PLACE(4)); W2STAGE(w2C, 4, PLACE(5));
        ST(6);
        lds_wait(sm, F_PART + j, 0, lane);
        W2STAGE(w2C, 5, PLACE(0)); W2STAGE(w2C, 6, PLACE(1));
        WAIT_V(16);
        W2READ(wr[4], PLACE(2)); W2READ(wr[5], PLACE(3));
        WAIT_L0(); SCHED();
        W2STAGE(w2C, 2, PLACE(2)); W2STAGE(w2C, 3, PLACE(3));
        ST(7);
        {
            const int kpl = ((7 * (w - 4 * ((lane >> 2) & 1))) >> 1) + (lane & 3);
            poll_flags(rs, WS_FH + ((rt >> 1) * 16 + (kpl >> 1)) * 128 + ((rt & 1) + 2 * (kpl & 1)) * 4, tag);
        }
        ST(11);
        v4u hbuf[7][4];
        f32x4 acc2[4][4] = {};
        lds_set(sm, F_W2OK + j, lane);
        __builtin_amdgcn_s_setprio(LPRIO);
        HLOAD(0); HLOAD(1); HLOAD(2); HLOAD(3);
        G2MMA(wr[0], 0); HLOAD(4); HLOAD(5);
        G2MMA(wr[1], 1); HLOAD(6);
        qv = *(const GAS v4u*)(q + (size_t)(64 * rt + es) * D + 64 * ct + ed);
        SCHED();
        s16x8 wl[4];
        G2MMA(wr[2], 2);
        G2MMA(wr[3], 3);
        G2MMA(wr[4], 4);
        W2READ(wl, PLACE(4)); SCHED(); G2MMA(wr[5], 5);
        G2MMA(wl, 6);
        ST(12);
        const int zoff = WS_Z + (64 * ct + 16 * j + qd) * 8192 + rt * 256 + c16 * 16;
        v4u z[4]; v2u gr;
#pragma unroll
        for (int i = 0; i < 4; ++i) z[i] = ld128(rs, zoff + i * 4 * 8192);
        gr = ld64(rs, WS_G + (64 * rt + lane) * 8);
        SCHED();
        lds_wait(sm, F_CONV, 3, lane);
#pragma unroll
        for (int s = 0; s < 4; ++s) {
            LAS char* pp = sm + (s < 3 ? L_CV + j * 16384 + s * 4096 : PLACE(4)) + c16 * 256;
#pragma unroll
            for (int n = 0; n < 4; ++n) *(LAS f32x4*)(pp + (((4 * n + qd) ^ c16) << 4)) = acc2[n][s];
        }
        SCHED();
#pragma unroll 1
        for (unsigned spins = 0; spins < SPIN_CAP; ++spins) {
            bool ok = gr.y == tag;
#pragma unroll
            for (int i = 0; i < 4; ++i) ok = ok && z[i].y == tag && z[i].w == tag;
            if (__all(ok)) break;
            __builtin_amdgcn_s_sleep(2);
            CBAR();
#pragma unroll
            for (int i = 0; i < 4; ++i) z[i] = ld128(rs, zoff + i * 4 * 8192);
            gr = ld64(rs, WS_G + (64 * rt + lane) * 8);
        }
#pragma unroll
        for (int i = 0; i < 4; ++i) *(LAS v2u*)(sm + L_CV + j * 16384 + 12288 + (4 * i + qd) * 128 + c16 * 8) = (v2u){z[i].x, z[i].z};
        if (j == 0) *(LAS unsigned*)(sm + L_CV + 12288 + 2048 + lane * 4) = gr.x;
        if (b == 255 && w == 5) {
#pragma unroll 1
            for (unsigned spins = 0; spins < SPIN_CAP; ++spins) {
                CBAR();
                bool ok = true;
#pragma unroll
                for (int i = 0; i < 4; ++i) ok = (ld32(rs, WS_FU + (i * 64 + lane) * 128) == tag) && ok;
                if (__all(ok)) break;
                __builtin_amdgcn_s_sleep(2);
            }
            if (lane == 0) st32(tag, rs, WS_E);
        }
    }
    WAIT_L0();
    ST(13);
    BAR();
    {
        const int zr = ed + qq;
        LAS char* zp = sm + L_CV + 12288 + (zr >> 4) * 16384 + (zr & 15) * 128 + 32 * j + 8 * p4;
        v2u za = __builtin_bit_cast(v2u, trread(zp)), zb = __builtin_bit_cast(v2u, trread(zp + 512));
        const float gg = *(LAS float*)(sm + L_CV + 12288 + 2048 + es * 4);
        const int sl0 = ((ed >> 2) ^ c16) << 4, sl1 = (((ed >> 2) + 1) ^ c16) << 4;
        LAS char* pp = sm + (j >> 1) * 32768 + (j & 1) * 4096 + c16 * 256;
        LAS char* pl = sm + (j < 3 ? L_CV + j * 4096 : 65536) + c16 * 256;
        const int lstr = j < 3 ? 16384 : 8192;
        f32x4 s0 = *(LAS f32x4*)(pp + sl0), s1 = *(LAS f32x4*)(pp + sl1);
#pragma unroll
        for (int i = 1; i < 4; ++i) { s0 += *(LAS f32x4*)(pp + i * 8192 + sl0); s1 += *(LAS f32x4*)(pp + i * 8192 + sl1); }
#pragma unroll
        for (int i = 0; i < 4; ++i) { s0 += *(LAS f32x4*)(pl + i * lstr + sl0); s1 += *(LAS f32x4*)(pl + i * lstr + sl1); }
        const float g1 = rbf(1.0f - gg);
        unsigned zw[4] = {za.x, za.y, zb.x, zb.y};
        float bs[8] = {s0[0], s0[1], s0[2], s0[3], s1[0], s1[1], s1[2], s1[3]};
        v4u o;
#pragma unroll
        for (int e = 0; e < 4; ++e) {
            unsigned bb = pk2(bs[2 * e], bs[2 * e + 1]);
            unsigned aa = pk2(bflo(qv[e]) * bflo(zw[e]), bfhi(qv[e]) * bfhi(zw[e]));
            unsigned ga = pk2(gg * bflo(aa), gg * bfhi(aa));
            unsigned gb = pk2(g1 * bflo(bb), g1 * bfhi(bb));
            o[e] = pk2(bflo(ga) + bflo(gb), bfhi(ga) + bfhi(gb));
        }
        *(GAS v4u*)(out + (size_t)(64 * rt + es) * D + 64 * ct + ed) = o;
    }
    asm volatile(".Lfused_end:" ::: "memory");
    ST(14);
    ST(15);
    ST_END(isG ? 0 : 1);
}

extern "C" void kernel_launch(void* const* d_in, const int* in_sizes, int n_in, void* d_out, int out_size, void* d_ws, size_t ws_size,
                              hipStream_t stream) {
    const u16* q = (const u16*)d_in[0]; const u16* k = (const u16*)d_in[1]; const u16* v = (const u16*)d_in[2];
    const u16* sk = (const u16*)d_in[3]; const u16* hk = (const u16*)d_in[4]; const u16* Dv = (const u16*)d_in[5];
    const u16* w1 = (const u16*)d_in[7]; const u16* w2 = (const u16*)d_in[8]; const u16* gw = (const u16*)d_in[9];
    fused<<<256, 512, 0, stream>>>(q, k, v, sk, hk, Dv, w1, w2, gw, (char*)d_ws, (u16*)d_out);
}
```

```cpp
#include <hip/hip_runtime.h>
#include <hip/hip_bf16.h>
#include <stdint.h>

#define LAS __attribute__((address_space(3)))
#define GAS __attribute__((address_space(1)))
typedef unsigned short u16;
typedef short s16x4 __attribute__((ext_vector_type(4)));
typedef short s16x8 __attribute__((ext_vector_type(8)));
typedef float f32x4 __attribute__((ext_vector_type(4)));
typedef unsigned v4u __attribute__((ext_vector_type(4)));
typedef unsigned v2u __attribute__((ext_vector_type(2)));
typedef __bf16 bf16x2_t __attribute__((ext_vector_type(2)));
typedef float f32x2 __attribute__((ext_vector_type(2)));
typedef __amdgpu_buffer_rsrc_t rsrc_t;

constexpr int S = 2048, D = 512, F = 1792;
#define PFA 3
#define PFB 4
#define HK2V 24
#define LPRIO 2
#define ZSLP 6
#define CALL 0
#define HKV 14

constexpr int WS_HF = 0;
constexpr int WS_UT = 8 << 20;
constexpr int WS_Z = 10 << 20;
constexpr int WS_G = 14 << 20;
constexpr int WS_FU = WS_G + 65536;
constexpr int WS_FH = WS_FU + 32768;
constexpr int WS_E = WS_FH + 32768;
constexpr int WS_BYTES = 16 << 20;
constexpr int SC1 = 16;
constexpr unsigned SPIN_CAP = 1u << 16;

__device__ __forceinline__ float bflo(unsigned w) { return __uint_as_float(w << 16); }
__device__ __forceinline__ float bfhi(unsigned w) { return __uint_as_float(w & 0xFFFF0000u); }
__device__ __forceinline__ unsigned pk2v(f32x2 f) { return __builtin_bit_cast(unsigned, __builtin_convertvector(f, bf16x2_t)); }
__device__ __forceinline__ unsigned pk2(float lo, float hi) { return pk2v((f32x2){lo, hi}); }
__device__ __forceinline__ float rbf(float f) { return bflo(pk2(f, f)); }
__device__ __forceinline__ s16x4 trread(LAS char* p) { return __builtin_amdgcn_ds_read_tr16_b64_v4i16((LAS s16x4*)p); }
__device__ __forceinline__ s16x8 cat8(s16x4 a, s16x4 b) { return __builtin_shufflevector(a, b, 0, 1, 2, 3, 4, 5, 6, 7); }
__device__ __forceinline__ void glds16(const void* base, unsigned voff, LAS char* l) {
    asm volatile("s_mov_b32 m0, %2\n\ts_nop 0\n\tglobal_load_lds_dwordx4 %0, %1"
                 :: "v"(voff), "s"(base), "s"((unsigned)(size_t)l) : "memory");
}
__device__ __forceinline__ void glds16x4(const void* base, unsigned vo0, unsigned vo1, unsigned vo2, unsigned vo3, LAS char* l) {
    asm volatile("s_mov_b32 m0, %5\n\ts_nop 0\n\tglobal_load_lds_dwordx4 %0, %4\n\tglobal_load_lds_dwordx4 %1, %4 offset:1024\n\t"
                 "global_load_lds_dwordx4 %2, %4 offset:2048\n\tglobal_load_lds_dwordx4 %3, %4 offset:3072"
                 :: "v"(vo0), "v"(vo1), "v"(vo2), "v"(vo3), "s"(base), "s"((unsigned)(size_t)l) : "memory");
}
#define WAIT_V(n) asm volatile("s_waitcnt vmcnt(" #n ")" ::: "memory")
#define WAIT_V0C() __builtin_amdgcn_s_waitcnt(0x0F70)
#define WAIT_L0() __builtin_amdgcn_s_waitcnt(0xC07F)
#define BAR() asm volatile("s_barrier" ::: "memory")
#define SCHED() __builtin_amdgcn_sched_barrier(0)
#define CBAR() asm volatile("" ::: "memory")
#define ST_DECL
#define ST(i)
#define ST_END(k)
#define STF(i)
#define MFMA(a, b, c) __builtin_amdgcn_mfma_f32_16x16x32_bf16(a, b, c, 0, 0, 0)

__device__ __forceinline__ v4u ld128(rsrc_t rs, int off) { return __builtin_amdgcn_raw_buffer_load_b128(rs, off, 0, SC1); }
__device__ __forceinline__ v2u ld64(rsrc_t rs, int off) { return __builtin_amdgcn_raw_buffer_load_b64(rs, off, 0, SC1); }
__device__ __forceinline__ unsigned ld32(rsrc_t rs, int off) { return __builtin_amdgcn_raw_buffer_load_b32(rs, off, 0, SC1); }
__device__ __forceinline__ void st128(v4u v, rsrc_t rs, int off) { __builtin_amdgcn_raw_buffer_store_b128(v, rs, off, 0, SC1); }
__device__ __forceinline__ void st64(v2u v, rsrc_t rs, int off) { __builtin_amdgcn_raw_buffer_store_b64(v, rs, off, 0, SC1); }
__device__ __forceinline__ void st32(unsigned v, rsrc_t rs, int off) { __builtin_amdgcn_raw_buffer_store_b32(v, rs, off, 0, SC1); }
__device__ __forceinline__ void poll_flags(rsrc_t rs, int off, unsigned tag) {
#pragma unroll 1
    for (unsigned spins = 0; spins < SPIN_CAP; ++spins) {
        CBAR();
        unsigned v = ld32(rs, off);
        if (__all(v == tag)) break;
        __builtin_amdgcn_s_sleep(4);
    }
    CBAR();
}
__device__ __forceinline__ v4u silu8(f32x4 a0, f32x4 a1) {
    f32x2 x[4] = {{a0[0], a0[1]}, {a0[2], a0[3]}, {a1[0], a1[1]}, {a1[2], a1[3]}};
    f32x2 t[4];
#pragma unroll
    for (int i = 0; i < 4; ++i) t[i] = x[i] * -1.44269504088896f;
#pragma unroll
    for (int i = 0; i < 4; ++i) { t[i].x = __builtin_amdgcn_exp2f(t[i].x); t[i].y = __builtin_amdgcn_exp2f(t[i].y); }
#pragma unroll
    for (int i = 0; i < 4; ++i) t[i] = t[i] + 1.0f;
#pragma unroll
    for (int i = 0; i < 4; ++i) { t[i].x = __builtin_amdgcn_rcpf(t[i].x); t[i].y = __builtin_amdgcn_rcpf(t[i].y); }
    v4u o;
#pragma unroll
    for (int i = 0; i < 4; ++i) o[i] = pk2v(x[i] * t[i]);
    return o;
}

constexpr int L_CV = 98304;
constexpr int L_KV = 131072;
constexpr int L_T = L_KV + 22528;
constexpr int U_CP = 4416, U_CH = 4 * U_CP, H_CP = 6400, H_CH = 2 * H_CP;
constexpr int L_U = L_CV, L_H = L_CV + 2 * U_CH;
constexpr int LDS_BYTES = 163840;
constexpr int L_ZS = L_H + 2 * H_CH;
constexpr int L_FL = LDS_BYTES - 128;
enum { F_W2OK = 0, F_C7 = 4, F_FILL = 8, F_CONV = 12, F_C01 = 16, F_PART = 20, F_TAG = 24, F_UT = 25 };

__device__ __forceinline__ void lds_set(LAS char* sm, int i, int lane, unsigned v = 1u) {
    if (lane == 0) *(volatile LAS unsigned*)(sm + L_FL + 4 * i) = v;
}
__device__ __forceinline__ void lds_wait(LAS char* sm, int i0, int mask, int lane, unsigned need = 1u) {
#pragma unroll 1
    for (unsigned spins = 0; spins < (1u << 22); ++spins) {
        unsigned v = *(volatile LAS unsigned*)(sm + L_FL + 4 * (i0 + (lane & mask)));
        if (__all(v >= need)) break;
        __builtin_amdgcn_s_sleep(1);
    }
    CBAR();
}

__device__ __forceinline__ v4u kv_prep(LAS char* kvw, int e, int lane, bool zero_halo) {
    const int gq = lane >> 4, qq = (lane & 15) >> 2, p = lane & 3;
    LAS char* kb = kvw + (8 * e + 4 + qq) * 128 + 32 * gq + 8 * p;
    v2u k0 = __builtin_bit_cast(v2u, trread(kb));
    v2u k1 = __builtin_bit_cast(v2u, trread(kb + 512));
    v2u k2 = __builtin_bit_cast(v2u, trread(kb + 1024));
    LAS char* vb = kvw + 3072 + (8 * e + qq) * 128 + 32 * gq + 8 * p;
    v2u v0 = __builtin_bit_cast(v2u, trread(vb));
    v2u v1 = __builtin_bit_cast(v2u, trread(vb + 512));
    v2u sk = *(LAS v2u*)(kvw + 5120 + 8 * lane);
    if (zero_halo) { k0.x = 0; k0.y = 0; }
    float kk[12] = {bflo(k0.x), bfhi(k0.x), bflo(k0.y), bfhi(k0.y), bflo(k1.x), bfhi(k1.x), bflo(k1.y), bfhi(k1.y),
                    bflo(k2.x), bfhi(k2.x), bflo(k2.y), bfhi(k2.y)};
    float vv[8] = {bflo(v0.x), bfhi(v0.x), bflo(v0.y), bfhi(v0.y), bflo(v1.x), bfhi(v1.x), bflo(v1.y), bfhi(v1.y)};
    const float w0 = bflo(sk.x), w1 = bfhi(sk.x), w2 = bflo(sk.y), w3 = bfhi(sk.y);
    float ks[8];
#pragma unroll
    for (int i = 0; i < 8; ++i) ks[i] = kk[i + 1] * w3 + kk[i + 2] * w2 + kk[i + 3] * w1 + kk[i + 4] * w0;
    float u[8];
#pragma unroll
    for (int i = 0; i < 8; i += 2) {
        unsigned r = pk2(ks[i], ks[i + 1]);
        u[i] = bflo(r) * vv[i];
        u[i + 1] = bfhi(r) * vv[i + 1];
    }
    return (v4u){pk2(u[7], u[6]), pk2(u[5], u[4]), pk2(u[3], u[2]), pk2(u[1], u[0])};
}

__device__ __forceinline__ unsigned ab16(unsigned hi, unsigned lo) { return __builtin_amdgcn_alignbit(hi, lo, 16); }
__device__ __forceinline__ void fill_u(LAS char* sm, v2u ua, v4u ubv, int ch, int L) {
    if (L == 0) { ua.x = 0; ua.y = 0; }
    const unsigned w0 = ua.x, w1 = ua.y, w2 = ubv.x, w3 = ubv.y, w4 = ubv.z, w5 = ubv.w;
    const unsigned t0 = ab16(w1, w0), t1 = ab16(w2, w1), t2 = ab16(w3, w2), t3 = ab16(w4, w3), t4 = ab16(w5, w4);
    LAS char* ub = sm + L_U + ch * U_CH;
    LAS char* up = ub + 16 * L + 64;
    *(LAS v4u*)(up) = (v4u){t1, t2, t3, t4};
    *(LAS v4u*)(up + U_CP) = (v4u){w1, w2, w3, w4};
    *(LAS v4u*)(up + 2 * U_CP) = (v4u){t0, t1, t2, t3};
    *(LAS v4u*)(up + 3 * U_CP) = (v4u){w0, w1, w2, w3};
    if (L == 255) {
        LAS char* e = ub + 16 * 256 + 64;
        *(LAS v4u*)(e) = (v4u){w5 >> 16, 0, 0, 0};
        *(LAS v4u*)(e + U_CP) = (v4u){w5, 0, 0, 0};
        *(LAS v4u*)(e + 2 * U_CP) = (v4u){t4, w5 >> 16, 0, 0};
        *(LAS v4u*)(e + 3 * U_CP) = (v4u){w4, w5, 0, 0};
    }
}
__device__ __forceinline__ void fill_h(LAS char* sm, v4u ha, v2u hbv, int ch, int L) {
    if (L == 255) { hbv.x = 0; hbv.y = 0; }
    LAS char* hb = sm + L_H + ch * H_CH;
    LAS char* hp = hb + ((L >> 3) + 8) * 160 + (L & 7) * 16;
    *(LAS v4u*)(hp) = ha;
    *(LAS v4u*)(hp + H_CP) = (v4u){ha.z, ha.w, hbv.x, hbv.y};
    if (L == 0) *(LAS v4u*)(hb + H_CP + 7 * 160 + 7 * 16) = (v4u){0, 0, ha.x, ha.y};
}
__device__ __forceinline__ void fill_pads_h(LAS char* sm, int ch, int half, int lane) {
    const v4u z = {0, 0, 0, 0};
    LAS char* hb = sm + L_H + ch * H_CH;
    if (!(half == 1 && lane == 63)) *(LAS v4u*)(hb + half * H_CP + (lane >> 3) * 160 + (lane & 7) * 16) = z;
}
__device__ __forceinline__ void fill_pads_u(LAS char* sm, int ch, int half, int lane) {
    const v4u z = {0, 0, 0, 0};
    if (lane < 16) {
        LAS char* ub = sm + L_U + ch * U_CH;
        const int c = 2 * half + (lane >> 3), jx = lane & 7;
        if (jx != 4) *(LAS v4u*)(ub + c * U_CP + (jx < 4 ? 16 * jx : 16 * (252 + jx) + 64)) = z;
    }
}

constexpr int conv_want(int t, int T0, int NA) {
    int wnt = (t + PFA < T0) ? t + PFA : (t + PFB > T0 - 1 ? t + PFB : T0 - 1);
    return t < -1 ? -1 : (wnt < NA - 1 ? wnt : NA - 1);
}
template <int I> struct IC { static constexpr int v = I; };
template <int A, int B, class Fn> __device__ __forceinline__ void static_for(Fn&& fn) {
    if constexpr (A < B) { fn(IC<A>{}); static_for<A + 1, B>(fn); }
}
template <int NTA, int NTB, class Hook>
__device__ __forceinline__ void conv_body(LAS char* ua, LAS char* hb, f32x4& aA0, f32x4& aA1, f32x4& aB0, f32x4& aB1, Hook hook) {
    constexpr int NA = 16 * NTA + 17, NB = 16 * NTB + 17, T0 = NA - NB;
    constexpr int HK = HKV, HK2 = HK2V;
    LAS char* up = ua - 64 * (NA - 1);
    asm volatile("" : "+v"(up), "+v"(hb));
    s16x8 W[NA + 1], BA[NA], BB[NB];
#define HOFF(t) (((t) & 1) ? (((t) + 1) / 2) * 160 : ((t) / 2) * 160 + 64)
    W[0] = *(LAS s16x8*)up;
    constexpr int PF = PFA;
    static_for<0, PF>([&](auto nc) __attribute__((always_inline)) {
        constexpr int nl = decltype(nc)::v;
        W[nl + 1] = *(LAS s16x8*)(up + 64 * (nl + 1));
        BA[nl] = *(LAS s16x8*)(hb + HOFF(nl));
    });
    SCHED();
    static_for<0, NA>([&](auto tc) __attribute__((always_inline)) {
        constexpr int t = decltype(tc)::v, nl = t + PF;
        aA1 = MFMA(W[t], BA[t], aA1);
        SCHED();
        if constexpr (nl + 1 < NA) W[nl + 1] = *(LAS s16x8*)(up + 64 * (nl + 1));
        SCHED();
        if constexpr (t + 1 < NA) aA0 = MFMA(W[t + 1], BA[t], aA0);
        SCHED();
        if constexpr (nl < NA) BA[nl] = *(LAS s16x8*)(hb + HOFF(nl));
        SCHED();
        if constexpr (t >= T0) { aB1 = MFMA(W[t], BB[t - T0], aB1); SCHED(); }
        if constexpr (nl >= T0 && nl < NA) { BB[nl - T0] = *(LAS s16x8*)(hb + HOFF(nl - T0)); SCHED(); }
        if constexpr (t >= T0 && t + 1 < NA) { aB0 = MFMA(W[t + 1], BB[t - T0], aB0); SCHED(); }
        if constexpr (t == HK) { hook(-1); SCHED(); }
    });
}

__global__ __launch_bounds__(512, 2) void fused(const u16* __restrict__ q, const u16* __restrict__ kin, const u16* __restrict__ vin,
                                                const u16* __restrict__ sk, const u16* __restrict__ hk, const u16* __restrict__ Dv,
                                                const u16* __restrict__ w1, const u16* __restrict__ w2, const u16* __restrict__ gw,
                                                char* __restrict__ ws, u16* __restrict__ out) {
    __shared__ __attribute__((aligned(16))) char smem[LDS_BYTES];
    LAS char* sm = (LAS char*)smem;
    const int b = blockIdx.x, x = b & 7, idx = b >> 3;
    const int tid = threadIdx.x, lane = tid & 63;
    const int w = __builtin_amdgcn_readfirstlane(tid >> 6);
    const int j = w & 3;
    const rsrc_t rs = __builtin_amdgcn_make_buffer_rsrc(ws, 0, WS_BYTES, 0x00020000);
    ST_DECL; ST(0);
    unsigned long long pc; unsigned len;
    asm volatile(".Lfused_pc:\n\ts_getpc_b64 %0\n\ts_mov_b32 %1, .Lfused_end-.Lfused_pc" : "=s"(pc), "=s"(len));
    unsigned ctch;
    {
        unsigned co = (unsigned)(idx * 8 + w) * 128u;
        co = co < len ? co : 0u;
        asm volatile("global_load_dword %0, %1, %2" : "=v"(ctch) : "v"(co), "s"(pc) : "memory");
    }
    asm volatile("" :: "s"(q), "s"(kin), "s"(vin), "s"(sk), "s"(hk), "s"(Dv), "s"(w1), "s"(w2), "s"(gw), "s"(ws), "s"(out));
    const int rt = 4 * x + (idx & 3), ct = idx >> 2;
    const int d0 = 64 * x + 2 * idx;
    const int c16 = lane & 15, qd = lane >> 4, qq = c16 >> 2, p4 = lane & 3;
    const bool isG = idx < 28;
    const int rp = 4 * (x >> 1) + (idx & 3), fp = 7 * (x & 1) + (idx >> 2);
    const int ch = j >> 1, half = j & 1;
    const int L0 = 128 * half + lane, L1 = L0 + 64;

#define PLACE(i) (((i) >> 1) * 32768 + j * 8192 + ((i) & 1) * 4096)
    const int w2rd = (4 * qd + qq) * 128 + 32 * ((2 * qd + (qq >> 1)) & 3) + 8 * p4;
#define W2READ(dst, addr)                                                                    \
    do {                                                                                     \
        _Pragma("unroll") for (int n_ = 0; n_ < 4; ++n_) {                                   \
            LAS char* a_ = sm + (addr) + (w2rd ^ (n_ << 5));                                 \
            dst[n_] = cat8(trread(a_), trread(a_ + 2048));                                   \
        }                                                                                    \
    } while (0)
    const unsigned w2off = (unsigned)((lane >> 3) * D + ((((((lane & 7) >> 1) ^ qd) << 1) | (lane & 1)) * 8)) * 2u;
    const u16* w2C = w2 + (size_t)(224 * j) * D + 64 * ct;
#define W2STAGE(base, k_, addr) glds16x4(base + (32 * (k_)) * D, w2off, w2off + 7168u, w2off + 14336u, w2off + 21504u, sm + (addr))
    const int h_off = WS_HF + (rt * 56 + 7 * w) * 4096 + lane * 16;
    const int es = 16 * j + c16, ed = 32 * (w >> 2) + 8 * qd;
    unsigned tag;
    v4u qv;
#define HLOAD(k_) do { _Pragma("unroll") for (int i_ = 0; i_ < 4; ++i_) hbuf[k_][i_] = ld128(rs, h_off + (k_) * 4096 + i_ * 1024); SCHED(); } while (0)
#define G2MMA(wf, k_) do { _Pragma("unroll") for (int n_ = 0; n_ < 4; ++n_) _Pragma("unroll") for (int s_ = 0; s_ < 4; ++s_) \
        acc2[n_][s_] = MFMA(wf[n_], __builtin_bit_cast(s16x8, hbuf[k_][s_]), acc2[n_][s_]); SCHED(); } while (0)
#define LPIECE(i) ((i) == 15 ? PLACE(4) + 2048 : L_CV + j * 16384 + (i) * 1024)

    unsigned ev = 0, dvr = 0, tch = 0;
    LAS char* kvw = sm + L_KV + j * 5632;
    if (w < 4) {
        const int r_ = 64 * idx + 16 * j - 8 + (lane >> 3);
        const unsigned co_ = (unsigned)(64 * x + (lane & 7) * 8) * 2u;
        glds16(kin, (unsigned)((r_ < 0 ? 0 : r_) * D) * 2u + co_, kvw);
        const unsigned o1_ = (unsigned)((r_ + 8) * D) * 2u + co_;
        glds16(kin, o1_, kvw + 1024);
        glds16(vin, o1_, kvw + 3072);
        if (lane < 32) glds16(sk, (unsigned)(256 * x + lane * 8) * 2u, kvw + 5120);
        glds16(kin + 8 * D, o1_, kvw + 2048);
        glds16(vin + 8 * D, o1_, kvw + 4096);
        asm volatile("global_load_dword %0, %2, %3 sc1\n\tglobal_load_ushort %1, %2, %4" : "=&v"(ev), "=&v"(dvr) : "v"(0), "s"(ws + WS_E), "s"(Dv + d0 + ch) : "memory");
        const int fpc = fp < 14 ? fp : 13, l5 = (lane & 31) + 32 * (j & 1);
        const int id = 128 + fpc * 64 + l5;
        const unsigned to = j < 2 ? (unsigned)((rp * 128 + (id & 127)) * 1024 + (id >> 7) * 128) : (unsigned)((32 * rp + (l5 >> 1)) * (F * 2) + fpc * 256 + (l5 & 1) * 128);
        asm volatile("global_load_dword %0, %1, %2" : "=v"(tch) : "v"(to), "s"(j < 2 ? (const char*)q : (const char*)w1) : "memory");
    }
    SCHED();

    if (w < 4) {
        unsigned dvs;
        if (w == 0 && lane < 32 && lane != F_TAG) *(LAS unsigned*)(sm + L_FL + 4 * lane) = 0u;
        const int sh = j & 1, fh = j >> 1;
        LAS char* bad[2][2]; LAS char* aad[2][4];
        {
            const int key = qq | ((qd & 1) << 2);
#pragma unroll
            for (int sp = 0; sp < 2; ++sp) {
                bad[sp][0] = sm + sp * 65536 + (64 * sh + c16) * 128 + 16 * (qd ^ (c16 >> 1));
                bad[sp][1] = sm + sp * 65536 + (64 * sh + c16) * 128 + 16 * ((4 + qd) ^ (c16 >> 1));
#pragma unroll
                for (int ft = 0; ft < 4; ++ft) aad[sp][ft] = sm + sp * 65536 + 16384 + (8 * qd + qq) * 256 + 32 * ((4 * fh + ft) ^ key) + 8 * p4;
                asm volatile("" : "+v"(bad[sp][0]), "+v"(bad[sp][1]), "+v"(aad[sp][0]), "+v"(aad[sp][1]), "+v"(aad[sp][2]), "+v"(aad[sp][3]));
            }
        }
        LAS char* tsrc = sm + L_T + (16 * j + (lane >> 3)) * 144 + (lane & 7) * 16;
        int tdst = WS_UT + (64 * x + 16 * j + (lane >> 3)) * 4096 + (1984 - 64 * idx) * 2 + (lane & 7) * 16;
        asm volatile("" : "+v"(tsrc), "+v"(tdst));
        WAIT_V(5);
        ST(1);
        *(LAS v4u*)(sm + L_T + lane * 144 + (7 - 2 * j) * 16) = kv_prep(kvw, 0, lane, idx == 0 && j == 0);
        SCHED(); WAIT_V(3);
        *(LAS v4u*)(sm + L_T + lane * 144 + (6 - 2 * j) * 16) = kv_prep(kvw, 1, lane, false);
        WAIT_L0(); SCHED();
        ST(2);
        BAR();
#define UT_STORE() do { v4u o0_ = *(LAS v4u*)tsrc, o1_ = *(LAS v4u*)(tsrc + 8 * 144); st128(o0_, rs, tdst); st128(o1_, rs, tdst + 8 * 4096); } while (0)
#define GET_TAG() do { \
            asm volatile("s_waitcnt vmcnt(0)\n\tv_readfirstlane_b32 %0, %2\n\tv_readfirstlane_b32 %1, %3\n\ts_nop 3" : "=s"(tag), "=s"(dvs) : "v"(ev), "v"(dvr), "v"(tch), "v"(ctch) : "memory"); \
            tag += 1u; \
            if (tid == 0) *(LAS unsigned*)(sm + L_FL + 4 * F_TAG) = tag; } while (0)
        const int fu_st = WS_FU + (x * 32 + idx) * 128;
        if (isG) {
            f32x4 acc[4][4] = {};
            s16x8 fa[2][2][4], fb[2][2][4];
#define READ(c)                                                                                     \
    do {                                                                                            \
        _Pragma("unroll") for (int kk = 0; kk < 2; ++kk) {                                          \
            _Pragma("unroll") for (int ft = 0; ft < 4; ++ft) {                                      \
                fb[(c) & 1][kk][ft] = *(LAS s16x8*)(bad[((c) >> 1) & 1][kk] + ((c) & 1) * 32768 + 2048 * ft); \
                LAS char* a_ = aad[((c) >> 1) & 1][ft] + ((c) & 1) * 32768 + kk * 8192;             \
                fa[(c) & 1][kk][ft] = cat8(trread(a_), trread(a_ + 1024));                          \
            }                                                                                       \
        }                                                                                           \
    } while (0)
#define MMA(c)                                                                                      \
    do {                                                                                            \
        _Pragma("unroll") for (int kk = 0; kk < 2; ++kk)                                            \
            _Pragma("unroll") for (int ft = 0; ft < 4; ++ft)                                        \
                _Pragma("unroll") for (int st = 0; st < 4; ++st)                                    \
                    acc[ft][st] = MFMA(fa[(c) & 1][kk][ft], fb[(c) & 1][kk][st], acc[ft][st]);      \
    } while (0)
#define ILV() do { _Pragma("unroll") for (int g_ = 0; g_ < 24; ++g_) { __builtin_amdgcn_sched_group_barrier(0x100, 1, 0); __builtin_amdgcn_sched_group_barrier(0x8, 1, 0); } \
                   __builtin_amdgcn_sched_group_barrier(0x8, 8, 0); SCHED(); } while (0)
            STF(1); SCHED(); UT_STORE(); READ(0); SCHED(); WAIT_L0(); STF(2);
            BAR(); STF(3); SCHED(); READ(1); MMA(0); ILV(); WAIT_L0(); STF(4);
            BAR(); STF(5); SCHED(); READ(2); MMA(1); ILV(); STF(6);
            GET_TAG(); WAIT_L0(); STF(7);
            BAR(); STF(8);
            if (tid == 0) st32(tag, rs, fu_st);
            SCHED(); READ(3); MMA(2); ILV(); WAIT_L0(); STF(9);
            ST(3);
            BAR(); STF(10); SCHED(); READ(4); MMA(3); ILV(); WAIT_L0(); STF(11);
            BAR(); STF(12); SCHED(); READ(5); MMA(4); ILV(); WAIT_L0(); STF(13);
            BAR(); STF(14); SCHED(); READ(6); MMA(5); ILV(); WAIT_L0();
            BAR(); STF(15); SCHED();
            ST(4);
            READ(7); MMA(6); ILV(); WAIT_L0();
            lds_set(sm, F_C7 + j, lane);
            SCHED();
            MMA(7);
            ST(5);
            const int hst = WS_HF + ((2 * rp + sh) * 56 + 4 * fp + 2 * fh) * 4096 + lane * 16;
#pragma unroll
            for (int pp = 0; pp < 2; ++pp)
#pragma unroll
                for (int st = 0; st < 4; ++st) st128(silu8(acc[2 * pp][st], acc[2 * pp + 1][st]), rs, hst + pp * 4096 + st * 1024);
            ST(6);
        } else {
            UT_STORE();
            const int r0 = 512 * (x >> 1) + 64 * (4 * (x & 1) + (idx - 28)) + 16 * j;
            v4u gv = *(const GAS v4u*)(gw + lane * 8);
            float gs[2];
#pragma unroll 1
            for (int hh = 0; hh < 2; ++hh) {
                v4u qr[8];
#pragma unroll
                for (int i = 0; i < 8; ++i) qr[i] = *(const GAS v4u*)(q + (size_t)(r0 + 8 * hh + i) * D + lane * 8);
                if (hh == 0) {
                    GET_TAG(); WAIT_L0();
                    BAR();
                    if (tid == 0) st32(tag, rs, fu_st);
                }
                float s[8];
#pragma unroll
                for (int i = 0; i < 8; ++i) {
                    s[i] = 0.f;
#pragma unroll
                    for (int e = 0; e < 4; ++e) {
                        unsigned r = pk2(bflo(qr[i][e]) * bflo(gv[e]), bfhi(qr[i][e]) * bfhi(gv[e]));
                        s[i] += bflo(r) + bfhi(r);
                    }
                }
#pragma unroll
                for (int i = 0; i < 4; ++i) { const bool hi = lane & 32; float snd = hi ? s[i] : s[i + 4], kp = hi ? s[i + 4] : s[i]; s[i] = kp + __shfl_xor(snd, 32); }
#pragma unroll
                for (int i = 0; i < 2; ++i) { const bool hi = lane & 16; float snd = hi ? s[i] : s[i + 2], kp = hi ? s[i + 2] : s[i]; s[i] = kp + __shfl_xor(snd, 16); }
                { const bool hi = lane & 8; float snd = hi ? s[0] : s[1], kp = hi ? s[1] : s[0]; s[0] = kp + __shfl_xor(snd, 8); }
                s[0] += __shfl_xor(s[0], 4); s[0] += __shfl_xor(s[0], 2); s[0] += __shfl_xor(s[0], 1);
                if (hh == 0) gs[0] = s[0]; else gs[1] = s[0];
            }
            if ((lane & 7) < 2) {
                float t = rbf((lane & 1) ? gs[1] : gs[0]);
                float g = rbf(__builtin_amdgcn_rcpf(1.0f + __builtin_amdgcn_exp2f(-1.44269504088896f * t)));
                st64((v2u){__float_as_uint(g), tag}, rs, WS_G + (r0 + 8 * (lane & 1) + (lane >> 3)) * 8);
            }
        }
#define H_PUBLISH() do { WAIT_V(0); if (isG && lane == 0) st32(tag, rs, WS_FH + (rp * 16 + fp) * 128 + j * 4); ST(8); } while (0)
        s16x8 wl[7][4];
        lds_wait(sm, F_C01 + j, 0, lane);
        W2READ(wl[0], PLACE(0)); W2READ(wl[1], PLACE(1));
        WAIT_L0(); SCHED();
        lds_set(sm, F_PART + j, lane);
        H_PUBLISH();
        lds_wait(sm, F_FILL + 2 * ch, 1, lane);
        CBAR();
        ST(7);
        f32x4 aA0 = {}, aA1 = {}, aB0 = {}, aB1 = {};
        const int b0 = c16 >> 3, b1 = c16 & 7;
        v2u uu[2][2];
        {
            LAS char* u3 = sm + L_U + ch * U_CH + 3 * U_CP + 64 + 2 * (2048 - (8 * qd + 4 * b0 + 64 * b1));
#pragma unroll
            for (int t = 0; t < 2; ++t)
#pragma unroll
                for (int mt = 0; mt < 2; ++mt) uu[t][mt] = *(LAS v2u*)(u3 - 1024 * (t ? half : 3 - half) - 64 * mt);
        }
        {
            LAS char* ua_ = sm + L_U + ch * U_CH + (c16 & 3) * U_CP + 64 + 4096 + 16 * (qd - (c16 >> 2));
            LAS char* hb_ = sm + L_H + ch * H_CH + (c16 >> 3) * H_CP + (c16 & 7) * 160 + qd * 16;
            auto hook = [&](int i) __attribute__((always_inline)) {};
            if (half == 0) conv_body<3, 0>(ua_, hb_, aA0, aA1, aB0, aB1, hook);
            else conv_body<2, 1>(ua_, hb_, aA0, aA1, aB0, aB1, hook);
        }
        ST(9);
        lds_set(sm, F_CONV + j, lane);
        {
            const float dd = bflo(dvs);
            const int srcl = ((lane >> 3) + 8 * (lane & 1) + 16 * ((lane >> 1) & 3)) * 4;
            const int zb = WS_Z + (d0 + ch) * 8192 + (lane >> 3) * 256 + (lane & 7) * 16;
#pragma unroll
            for (int t = 0; t < 2; ++t)
#pragma unroll
                for (int mt = 0; mt < 2; ++mt) {
                    f32x4 y = t ? (mt ? aB1 : aB0) : (mt ? aA1 : aA0);
                    v2u u_ = uu[t][mt];
                    unsigned y01 = pk2(y[0], y[1]), y23 = pk2(y[2], y[3]);
                    unsigned p01 = pk2(bfhi(u_.y) * dd, bflo(u_.y) * dd), p23 = pk2(bfhi(u_.x) * dd, bflo(u_.x) * dd);
                    unsigned z0 = pk2(bflo(y01) + bflo(p01), bfhi(y01) + bfhi(p01));
                    unsigned z1 = pk2(bflo(y23) + bflo(p23), bfhi(y23) + bfhi(p23));
                    z0 = __builtin_amdgcn_ds_bpermute(srcl, z0);
                    z1 = __builtin_amdgcn_ds_bpermute(srcl, z1);
                    st128((v4u){z0, tag, z1, tag}, rs, zb + (t ? half : 3 - half) * 2048 + mt * 128);
                }
            SCHED();
        }
        ST(10);
        lds_wait(sm, F_W2OK + j, 0, lane);
        ST(11);
        v4u hbuf[7][4];
        f32x4 acc2[4][4] = {};
        HLOAD(0); HLOAD(1); HLOAD(2); HLOAD(3);
        if (!CALL) G2MMA(wl[0], 0);
        HLOAD(4); HLOAD(5); HLOAD(6);
        qv = *(const GAS v4u*)(q + (size_t)(64 * rt + es) * D + 64 * ct + ed);
        lds_set(sm, F_C7 + j, lane, 2u);
        SCHED();
        if (CALL) G2MMA(wl[0], 0);
        W2READ(wl[2], PLACE(2)); SCHED(); G2MMA(wl[1], 1);
        W2READ(wl[3], PLACE(3)); SCHED(); G2MMA(wl[2], 2);
        W2READ(wl[4], PLACE(5)); SCHED(); G2MMA(wl[3], 3);
        W2READ(wl[5], PLACE(0)); SCHED(); G2MMA(wl[4], 4);
        W2READ(wl[6], PLACE(1)); SCHED(); G2MMA(wl[5], 5);
        G2MMA(wl[6], 6);
        ST(12);
#pragma unroll
        for (int s = 0; s < 4; ++s) {
            LAS char* pp = sm + PLACE(s) + c16 * 256;
#pragma unroll
            for (int n = 0; n < 4; ++n) *(LAS f32x4*)(pp + (((4 * n + qd) ^ c16) << 4)) = acc2[n][s];
        }
    } else {
        const int fu_off = WS_FU + (x * 32 + (lane & 31)) * 128;
        const u16* w2L = w2 + (size_t)(224 * w) * D + 64 * ct;
        const unsigned xs0 = (unsigned)((rp * 128 + 32 * j + (lane >> 3)) * D + (((lane & 7) ^ qd) * 8)) * 2u;
        const unsigned xs1 = (unsigned)((rp * 128 + 32 * j + (lane >> 3)) * D + (((lane & 7) ^ (4 + qd)) * 8)) * 2u + 7168u;
        const unsigned ws0 = (unsigned)((16 * j + qd) * F + fp * 128 + (((((c16 >> 1) ^ qd) << 1) | (lane & 1)) * 8)) * 2u;
        const unsigned ws1 = (unsigned)((16 * j + qd) * F + fp * 128 + (((((c16 >> 1) ^ (qd | 4)) << 1) | (lane & 1)) * 8)) * 2u + 2u * 13312u;
#define STAGE(c)                                                                                   \
    do {                                                                                           \
        LAS char* sl = sm + ((c) & 3) * 32768 + 4096 * j;                                          \
        glds16x4(q + (c) * 64, xs0, xs1, xs0 + 14336u, xs1 + 14336u, sl);                          \
        glds16x4(w1 + (c) * 64 * F, ws0, ws0 + 13312u, ws1, ws1 + 13312u, sl + 16384);             \
    } while (0)
        const char* hrow = (const char*)hk + (size_t)(d0 + ch) * 4096;
        const int urow = WS_UT + (d0 + ch) * 4096;
        v4u ha0, ha1, ub0, ub1; v2u hb0, hb1, ua0, ua1;
#define HKLOAD() asm volatile("global_load_dwordx4 %0, %4, %6\n\tglobal_load_dwordx2 %1, %4, %6 offset:16\n\t" \
                     "global_load_dwordx4 %2, %4, %6 offset:1024\n\tglobal_load_dwordx2 %3, %5, %6" \
                     : "=&v"(ha0), "=&v"(hb0), "=&v"(ha1), "=&v"(hb1) \
                     : "v"(16 * L0), "v"(L1 == 255 ? 0 : 16 * L1 + 16), "s"(hrow) : "memory")
        if (isG) {
            STAGE(0); STAGE(1); STAGE(2);
            ST(1);
            asm volatile("s_waitcnt vmcnt(16)" :: "v"(ctch) : "memory"); BAR(); STAGE(3);
            ST(2);
            WAIT_V(16); BAR(); STAGE(4);
            WAIT_V(16); BAR(); STAGE(5);
            WAIT_V(16); BAR(); STAGE(6);
            ST(3);
            WAIT_V(16); BAR(); STAGE(7);
        } else {
            unsigned t0_, t1_, t2_, t3_;
            const unsigned wo = (unsigned)((((idx - 28) * 8 + x) * 4 + j) * 112 + lane) * 128u;
            asm volatile("global_load_dword %0, %2, %4 sc1\n\tglobal_load_dword %1, %3, %5 sc1"
                         : "=&v"(t0_), "=&v"(t1_) : "v"((unsigned)lane << 18), "v"((unsigned)(lane & 7) << 18), "s"(ws), "s"(out) : "memory");
            BAR();
            asm volatile("global_load_dword %0, %2, %4\n\tglobal_load_dword %1, %3, %4" : "=&v"(t2_), "=&v"(t3_) : "v"(wo), "v"(wo + (lane < 48 ? 8192u : 0u)), "s"(w2) : "memory");
            BAR();
            asm volatile("s_waitcnt vmcnt(0)" :: "v"(t0_), "v"(t1_), "v"(t2_), "v"(t3_), "v"(ctch) : "memory");
            __builtin_amdgcn_s_sleep(20);
        }
        HKLOAD();
        tag = __builtin_amdgcn_readfirstlane(*(volatile LAS unsigned*)(sm + L_FL + 4 * F_TAG));
        if (isG) { WAIT_V(20); BAR(); }
        unsigned pr = 0;
        if (j == 0) asm volatile("global_load_dword %0, %1, %2 sc1" : "=v"(pr) : "v"(fu_off), "s"(ws) : "memory");
        if (isG) { WAIT_V(12); BAR(); }
        W2STAGE(w2C, 0, PLACE(0)); W2STAGE(w2C, 1, PLACE(1));
        asm volatile("s_waitcnt vmcnt(8)" : "+v"(pr), "+v"(ha0), "+v"(hb0), "+v"(ha1), "+v"(hb1) :: "memory");
        const bool utok = j != 0 || __all(pr == tag);
        if (j == 0 && utok) lds_set(sm, F_UT, lane);
        if (isG) { WAIT_L0(); BAR(); }
        ST(4);
        if (!utok) { poll_flags(rs, fu_off, tag); lds_set(sm, F_UT, lane); }
        lds_wait(sm, F_UT, 0, lane);
        ST(8);
        asm volatile("global_load_dwordx2 %0, %4, %6 sc1\n\tglobal_load_dwordx4 %1, %5, %6 sc1\n\t"
                     "global_load_dwordx2 %2, %5, %6 offset:1016 sc1\n\tglobal_load_dwordx4 %3, %5, %6 offset:1024 sc1"
                     : "=&v"(ua0), "=&v"(ub0), "=&v"(ua1), "=&v"(ub1)
                     : "v"(urow + (L0 == 0 ? 0 : 16 * L0 - 8)), "v"(urow + 16 * L0), "s"(ws) : "memory");
        W2STAGE(w2L, 0, PLACE(2)); W2STAGE(w2L, 1, PLACE(3));
        __builtin_amdgcn_s_setprio(2);
        fill_pads_h(sm, ch, half, lane);
        fill_h(sm, ha0, hb0, ch, L0); fill_h(sm, ha1, hb1, ch, L1);
        SCHED();
        __builtin_amdgcn_s_setprio(0);
        W2STAGE(w2L, 2, PLACE(4)); W2STAGE(w2L, 3, PLACE(5));
        __builtin_amdgcn_s_setprio(2);
        if (isG) lds_wait(sm, F_C7, 3, lane);
        asm volatile("s_waitcnt vmcnt(16)" : "+v"(ua0), "+v"(ub0), "+v"(ua1), "+v"(ub1) :: "memory");
        lds_set(sm, F_C01 + j, lane);
        ST(9);
        fill_pads_u(sm, ch, half, lane);
        fill_u(sm, ua0, ub0, ch, L0); fill_u(sm, ua1, ub1, ch, L1);
        WAIT_L0(); SCHED();
        lds_set(sm, F_FILL + j, lane);
        __builtin_amdgcn_s_setprio(0);
        ST(5);
        s16x8 wr[6][4];
        WAIT_V(8);
        W2READ(wr[0], PLACE(2)); W2READ(wr[1], PLACE(3));
        WAIT_L0(); SCHED();
        W2STAGE(w2L, 4, PLACE(2)); W2STAGE(w2L, 5, PLACE(3));
        WAIT_V(8);
        W2READ(wr[2], PLACE(4)); W2READ(wr[3], PLACE(5));
        WAIT_L0(); SCHED();
        W2STAGE(w2L, 6, PLACE(4)); W2STAGE(w2C, 4, PLACE(5));
        ST(6);
        lds_wait(sm, F_PART + j, 0, lane);
        W2STAGE(w2C, 5, PLACE(0)); W2STAGE(w2C, 6, PLACE(1));
        WAIT_V(16);
        W2READ(wr[4], PLACE(2)); W2READ(wr[5], PLACE(3));
        WAIT_L0(); SCHED();
        W2STAGE(w2C, 2, PLACE(2)); W2STAGE(w2C, 3, PLACE(3));
        ST(7);
        {
            const int kpl = ((7 * (w - 4 * ((lane >> 2) & 1))) >> 1) + (lane & 3);
            poll_flags(rs, WS_FH + ((rt >> 1) * 16 + (kpl >> 1)) * 128 + ((rt & 1) + 2 * (kpl & 1)) * 4, tag);
        }
        ST(11);
        v4u hbuf[7][4];
        f32x4 acc2[4][4] = {};
        lds_set(sm, F_W2OK + j, lane);
        __builtin_amdgcn_s_setprio(LPRIO);
        HLOAD(0); HLOAD(1); HLOAD(2); HLOAD(3);
        G2MMA(wr[0], 0); HLOAD(4); HLOAD(5);
        G2MMA(wr[1], 1); HLOAD(6);
        qv = *(const GAS v4u*)(q + (size_t)(64 * rt + es) * D + 64 * ct + ed);
        SCHED();
        s16x8 wl[4];
        G2MMA(wr[2], 2);
        G2MMA(wr[3], 3);
        G2MMA(wr[4], 4);
        W2READ(wl, PLACE(4)); SCHED(); G2MMA(wr[5], 5);
        G2MMA(wl, 6);
        ST(12);
        __builtin_amdgcn_s_setprio(0);
        lds_wait(sm, F_CONV, 3, lane);
#pragma unroll
        for (int s = 0; s < 4; ++s) {
            LAS char* pp = sm + (s < 3 ? L_CV + j * 16384 + s * 4096 : PLACE(4)) + c16 * 256;
#pragma unroll
            for (int n = 0; n < 4; ++n) *(LAS f32x4*)(pp + (((4 * n + qd) ^ c16) << 4)) = acc2[n][s];
        }
        SCHED();
        lds_wait(sm, F_C7 + j, 0, lane, 2u);
        const int zoff = WS_Z + (64 * ct + 16 * j + qd) * 8192 + rt * 256 + c16 * 16;
        v4u z[4]; v2u gr;
#pragma unroll 1
        for (unsigned spins = 0; spins < SPIN_CAP; ++spins) {
            CBAR();
#pragma unroll
            for (int i = 0; i < 4; ++i) z[i] = ld128(rs, zoff + i * 4 * 8192);
            gr = ld64(rs, WS_G + (64 * rt + lane) * 8);
            bool ok = gr.y == tag;
#pragma unroll
            for (int i = 0; i < 4; ++i) ok = ok && z[i].y == tag && z[i].w == tag;
            if (__all(ok)) break;
            __builtin_amdgcn_s_sleep(ZSLP);
        }
#pragma unroll
        for (int i = 0; i < 4; ++i) *(LAS v2u*)(sm + L_CV + j * 16384 + 12288 + (4 * i + qd) * 128 + c16 * 8) = (v2u){z[i].x, z[i].z};
        if (j == 0) *(LAS unsigned*)(sm + L_CV + 12288 + 2048 + lane * 4) = gr.x;
        if (b == 255 && w == 5) {
#pragma unroll 1
            for (unsigned spins = 0; spins < SPIN_CAP; ++spins) {
                CBAR();
                bool ok = true;
#pragma unroll
                for (int i = 0; i < 4; ++i) ok = (ld32(rs, WS_FU + (i * 64 + lane) * 128) == tag) && ok;
                if (__all(ok)) break;
                __builtin_amdgcn_s_sleep(2);
            }
            if (lane == 0) st32(tag, rs, WS_E);
        }
    }
    WAIT_L0();
    ST(13);
    BAR();
    {
        const int zr = ed + qq;
        LAS char* zp = sm + L_CV + 12288 + (zr >> 4) * 16384 + (zr & 15) * 128 + 32 * j + 8 * p4;
        v2u za = __builtin_bit_cast(v2u, trread(zp)), zb = __builtin_bit_cast(v2u, trread(zp + 512));
        const float gg = *(LAS float*)(sm + L_CV + 12288 + 2048 + es * 4);
        const int sl0 = ((ed >> 2) ^ c16) << 4, sl1 = (((ed >> 2) + 1) ^ c16) << 4;
        LAS char* pp = sm + (j >> 1) * 32768 + (j & 1) * 4096 + c16 * 256;
        LAS char* pl = sm + (j < 3 ? L_CV + j * 4096 : 65536) + c16 * 256;
        const int lstr = j < 3 ? 16384 : 8192;
        f32x4 s0 = *(LAS f32x4*)(pp + sl0), s1 = *(LAS f32x4*)(pp + sl1);
#pragma unroll
        for (int i = 1; i < 4; ++i) { s0 += *(LAS f32x4*)(pp + i * 8192 + sl0); s1 += *(LAS f32x4*)(pp + i * 8192 + sl1); }
#pragma unroll
        for (int i = 0; i < 4; ++i) { s0 += *(LAS f32x4*)(pl + i * lstr + sl0); s1 += *(LAS f32x4*)(pl + i * lstr + sl1); }
        const float g1 = rbf(1.0f - gg);
        unsigned zw[4] = {za.x, za.y, zb.x, zb.y};
        float bs[8] = {s0[0], s0[1], s0[2], s0[3], s1[0], s1[1], s1[2], s1[3]};
        v4u o;
#pragma unroll
        for (int e = 0; e < 4; ++e) {
            unsigned bb = pk2(bs[2 * e], bs[2 * e + 1]);
            unsigned aa = pk2(bflo(qv[e]) * bflo(zw[e]), bfhi(qv[e]) * bfhi(zw[e]));
            unsigned ga = pk2(gg * bflo(aa), gg * bfhi(aa));
            unsigned gb = pk2(g1 * bflo(bb), g1 * bfhi(bb));
            o[e] = pk2(bflo(ga) + bflo(gb), bfhi(ga) + bfhi(gb));
        }
        *(GAS v4u*)(out + (size_t)(64 * rt + es) * D + 64 * ct + ed) = o;
    }
    asm volatile(".Lfused_end:" ::: "memory");
    ST(14);
    ST(15);
    ST_END(isG ? 0 : 1);
}

extern "C" void kernel_launch(void* const* d_in, const int* in_sizes, int n_in, void* d_out, int out_size, void* d_ws, size_t ws_size,
                              hipStream_t stream) {
    const u16* q = (const u16*)d_in[0]; const u16* k = (const u16*)d_in[1]; const u16* v = (const u16*)d_in[2];
    const u16* sk = (const u16*)d_in[3]; const u16* hk = (const u16*)d_in[4]; const u16* Dv = (const u16*)d_in[5];
    const u16* w1 = (const u16*)d_in[7]; const u16* w2 = (const u16*)d_in[8]; const u16* gw = (const u16*)d_in[9];
    fused<<<256, 512, 0, stream>>>(q, k, v, sk, hk, Dv, w1, w2, gw, (char*)d_ws, (u16*)d_out);
}
```

```cpp
#include <hip/hip_runtime.h>
#include <hip/hip_bf16.h>
#include <stdint.h>

#define LAS __attribute__((address_space(3)))
#define GAS __attribute__((address_space(1)))
typedef unsigned short u16;
typedef short s16x4 __attribute__((ext_vector_type(4)));
typedef short s16x8 __attribute__((ext_vector_type(8)));
typedef float f32x4 __attribute__((ext_vector_type(4)));
typedef unsigned v4u __attribute__((ext_vector_type(4)));
typedef unsigned v2u __attribute__((ext_vector_type(2)));
typedef __bf16 bf16x2_t __attribute__((ext_vector_type(2)));
typedef float f32x2 __attribute__((ext_vector_type(2)));
typedef __amdgpu_buffer_rsrc_t rsrc_t;

constexpr int S = 2048, D = 512, F = 1792;
#define PFA 3
#define PFB 4
#define HK2V 24
#define LPRIO 2
#define ZSLP 6
#define CALL 0
#define NTS " nt"
#define NTH " nt"
#define HKV 14

constexpr int WS_HF = 0;
constexpr int WS_UT = 8 << 20;
constexpr int WS_Z = 10 << 20;
constexpr int WS_G = 14 << 20;
constexpr int WS_FU = WS_G + 65536;
constexpr int WS_FH = WS_FU + 32768;
constexpr int WS_E = WS_FH + 32768;
constexpr int WS_BYTES = 16 << 20;
constexpr int SC1 = 16;
constexpr unsigned SPIN_CAP = 1u << 16;

__device__ __forceinline__ float bflo(unsigned w) { return __uint_as_float(w << 16); }
__device__ __forceinline__ float bfhi(unsigned w) { return __uint_as_float(w & 0xFFFF0000u); }
__device__ __forceinline__ unsigned pk2v(f32x2 f) { return __builtin_bit_cast(unsigned, __builtin_convertvector(f, bf16x2_t)); }
__device__ __forceinline__ unsigned pk2(float lo, float hi) { return pk2v((f32x2){lo, hi}); }
__device__ __forceinline__ float rbf(float f) { return bflo(pk2(f, f)); }
__device__ __forceinline__ s16x4 trread(LAS char* p) { return __builtin_amdgcn_ds_read_tr16_b64_v4i16((LAS s16x4*)p); }
__device__ __forceinline__ s16x8 cat8(s16x4 a, s16x4 b) { return __builtin_shufflevector(a, b, 0, 1, 2, 3, 4, 5, 6, 7); }
__device__ __forceinline__ void glds16(const void* base, unsigned voff, LAS char* l) {
    asm volatile("s_mov_b32 m0, %2\n\ts_nop 0\n\tglobal_load_lds_dwordx4 %0, %1" NTS
                 :: "v"(voff), "s"(base), "s"((unsigned)(size_t)l) : "memory");
}
__device__ __forceinline__ void glds16x4(const void* base, unsigned vo0, unsigned vo1, unsigned vo2, unsigned vo3, LAS char* l) {
    asm volatile("s_mov_b32 m0, %5\n\ts_nop 0\n\tglobal_load_lds_dwordx4 %0, %4\n\tglobal_load_lds_dwordx4 %1, %4 offset:1024\n\t"
                 "global_load_lds_dwordx4 %2, %4 offset:2048\n\tglobal_load_lds_dwordx4 %3, %4 offset:3072"
                 :: "v"(vo0), "v"(vo1), "v"(vo2), "v"(vo3), "s"(base), "s"((unsigned)(size_t)l) : "memory");
}
#define WAIT_V(n) asm volatile("s_waitcnt vmcnt(" #n ")" ::: "memory")
#define WAIT_V0C() __builtin_amdgcn_s_waitcnt(0x0F70)
#define WAIT_L0() __builtin_amdgcn_s_waitcnt(0xC07F)
#define BAR() asm volatile("s_barrier" ::: "memory")
#define SCHED() __builtin_amdgcn_sched_barrier(0)
#define CBAR() asm volatile("" ::: "memory")
#define ST_DECL
#define ST(i)
#define ST_END(k)
#define STF(i)
#define MFMA(a, b, c) __builtin_amdgcn_mfma_f32_16x16x32_bf16(a, b, c, 0, 0, 0)

__device__ __forceinline__ v4u ld128(rsrc_t rs, int off) { return __builtin_amdgcn_raw_buffer_load_b128(rs, off, 0, SC1); }
__device__ __forceinline__ v2u ld64(rsrc_t rs, int off) { return __builtin_amdgcn_raw_buffer_load_b64(rs, off, 0, SC1); }
__device__ __forceinline__ unsigned ld32(rsrc_t rs, int off) { return __builtin_amdgcn_raw_buffer_load_b32(rs, off, 0, SC1); }
__device__ __forceinline__ void st128(v4u v, rsrc_t rs, int off) { __builtin_amdgcn_raw_buffer_store_b128(v, rs, off, 0, SC1); }
__device__ __forceinline__ void st64(v2u v, rsrc_t rs, int off) { __builtin_amdgcn_raw_buffer_store_b64(v, rs, off, 0, SC1); }
__device__ __forceinline__ void st32(unsigned v, rsrc_t rs, int off) { __builtin_amdgcn_raw_buffer_store_b32(v, rs, off, 0, SC1); }
__device__ __forceinline__ void poll_flags(rsrc_t rs, int off, unsigned tag) {
#pragma unroll 1
    for (unsigned spins = 0; spins < SPIN_CAP; ++spins) {
        CBAR();
        unsigned v = ld32(rs, off);
        if (__all(v == tag)) break;
        __builtin_amdgcn_s_sleep(4);
    }
    CBAR();
}
__device__ __forceinline__ v4u silu8(f32x4 a0, f32x4 a1) {
    f32x2 x[4] = {{a0[0], a0[1]}, {a0[2], a0[3]}, {a1[0], a1[1]}, {a1[2], a1[3]}};
    f32x2 t[4];
#pragma unroll
    for (int i = 0; i < 4; ++i) t[i] = x[i] * -1.44269504088896f;
#pragma unroll
    for (int i = 0; i < 4; ++i) { t[i].x = __builtin_amdgcn_exp2f(t[i].x); t[i].y = __builtin_amdgcn_exp2f(t[i].y); }
#pragma unroll
    for (int i = 0; i < 4; ++i) t[i] = t[i] + 1.0f;
#pragma unroll
    for (int i = 0; i < 4; ++i) { t[i].x = __builtin_amdgcn_rcpf(t[i].x); t[i].y = __builtin_amdgcn_rcpf(t[i].y); }
    v4u o;
#pragma unroll
    for (int i = 0; i < 4; ++i) o[i] = pk2v(x[i] * t[i]);
    return o;
}

constexpr int L_CV = 98304;
constexpr int L_KV = 131072;
constexpr int L_T = L_KV + 22528;
constexpr int U_CP = 4416, U_CH = 4 * U_CP, H_CP = 6400, H_CH = 2 * H_CP;
constexpr int L_U = L_CV, L_H = L_CV + 2 * U_CH;
constexpr int LDS_BYTES = 163840;
constexpr int L_ZS = L_H + 2 * H_CH;
constexpr int L_FL = LDS_BYTES - 128;
enum { F_W2OK = 0, F_C7 = 4, F_FILL = 8, F_CONV = 12, F_C01 = 16, F_PART = 20, F_TAG = 24, F_UT = 25 };

__device__ __forceinline__ void lds_set(LAS char* sm, int i, int lane, unsigned v = 1u) {
    if (lane == 0) *(volatile LAS unsigned*)(sm + L_FL + 4 * i) = v;
}
__device__ __forceinline__ void lds_wait(LAS char* sm, int i0, int mask, int lane, unsigned need = 1u) {
#pragma unroll 1
    for (unsigned spins = 0; spins < (1u << 22); ++spins) {
        unsigned v = *(volatile LAS unsigned*)(sm + L_FL + 4 * (i0 + (lane & mask)));
        if (__all(v >= need)) break;
        __builtin_amdgcn_s_sleep(1);
    }
    CBAR();
}

__device__ __forceinline__ v4u kv_prep(LAS char* kvw, int e, int lane, bool zero_halo) {
    const int gq = lane >> 4, qq = (lane & 15) >> 2, p = lane & 3;
    LAS char* kb = kvw + (8 * e + 4 + qq) * 128 + 32 * gq + 8 * p;
    v2u k0 = __builtin_bit_cast(v2u, trread(kb));
    v2u k1 = __builtin_bit_cast(v2u, trread(kb + 512));
    v2u k2 = __builtin_bit_cast(v2u, trread(kb + 1024));
    LAS char* vb = kvw + 3072 + (8 * e + qq) * 128 + 32 * gq + 8 * p;
    v2u v0 = __builtin_bit_cast(v2u, trread(vb));
    v2u v1 = __builtin_bit_cast(v2u, trread(vb + 512));
    v2u sk = *(LAS v2u*)(kvw + 5120 + 8 * lane);
    if (zero_halo) { k0.x = 0; k0.y = 0; }
    float kk[12] = {bflo(k0.x), bfhi(k0.x), bflo(k0.y), bfhi(k0.y), bflo(k1.x), bfhi(k1.x), bflo(k1.y), bfhi(k1.y),
                    bflo(k2.x), bfhi(k2.x), bflo(k2.y), bfhi(k2.y)};
    float vv[8] = {bflo(v0.x), bfhi(v0.x), bflo(v0.y), bfhi(v0.y), bflo(v1.x), bfhi(v1.x), bflo(v1.y), bfhi(v1.y)};
    const float w0 = bflo(sk.x), w1 = bfhi(sk.x), w2 = bflo(sk.y), w3 = bfhi(sk.y);
    float ks[8];
#pragma unroll
    for (int i = 0; i < 8; ++i) ks[i] = kk[i + 1] * w3 + kk[i + 2] * w2 + kk[i + 3] * w1 + kk[i + 4] * w0;
    float u[8];
#pragma unroll
    for (int i = 0; i < 8; i += 2) {
        unsigned r = pk2(ks[i], ks[i + 1]);
        u[i] = bflo(r) * vv[i];
        u[i + 1] = bfhi(r) * vv[i + 1];
    }
    return (v4u){pk2(u[7], u[6]), pk2(u[5], u[4]), pk2(u[3], u[2]), pk2(u[1], u[0])};
}

__device__ __forceinline__ unsigned ab16(unsigned hi, unsigned lo) { return __builtin_amdgcn_alignbit(hi, lo, 16); }
__device__ __forceinline__ void fill_u(LAS char* sm, v2u ua, v4u ubv, int ch, int L) {
    if (L == 0) { ua.x = 0; ua.y = 0; }
    const unsigned w0 = ua.x, w1 = ua.y, w2 = ubv.x, w3 = ubv.y, w4 = ubv.z, w5 = ubv.w;
    const unsigned t0 = ab16(w1, w0), t1 = ab16(w2, w1), t2 = ab16(w3, w2), t3 = ab16(w4, w3), t4 = ab16(w5, w4);
    LAS char* ub = sm + L_U + ch * U_CH;
    LAS char* up = ub + 16 * L + 64;
    *(LAS v4u*)(up) = (v4u){t1, t2, t3, t4};
    *(LAS v4u*)(up + U_CP) = (v4u){w1, w2, w3, w4};
    *(LAS v4u*)(up + 2 * U_CP) = (v4u){t0, t1, t2, t3};
    *(LAS v4u*)(up + 3 * U_CP) = (v4u){w0, w1, w2, w3};
    if (L == 255) {
        LAS char* e = ub + 16 * 256 + 64;
        *(LAS v4u*)(e) = (v4u){w5 >> 16, 0, 0, 0};
        *(LAS v4u*)(e + U_CP) = (v4u){w5, 0, 0, 0};
        *(LAS v4u*)(e + 2 * U_CP) = (v4u){t4, w5 >> 16, 0, 0};
        *(LAS v4u*)(e + 3 * U_CP) = (v4u){w4, w5, 0, 0};
    }
}
__device__ __forceinline__ void fill_h(LAS char* sm, v4u ha, v2u hbv, int ch, int L) {
    if (L == 255) { hbv.x = 0; hbv.y = 0; }
    LAS char* hb = sm + L_H + ch * H_CH;
    LAS char* hp = hb + ((L >> 3) + 8) * 160 + (L & 7) * 16;
    *(LAS v4u*)(hp) = ha;
    *(LAS v4u*)(hp + H_CP) = (v4u){ha.z, ha.w, hbv.x, hbv.y};
    if (L == 0) *(LAS v4u*)(hb + H_CP + 7 * 160 + 7 * 16) = (v4u){0, 0, ha.x, ha.y};
}
__device__ __forceinline__ void fill_pads_h(LAS char* sm, int ch, int half, int lane) {
    const v4u z = {0, 0, 0, 0};
    LAS char* hb = sm + L_H + ch * H_CH;
    if (!(half == 1 && lane == 63)) *(LAS v4u*)(hb + half * H_CP + (lane >> 3) * 160 + (lane & 7) * 16) = z;
}
__device__ __forceinline__ void fill_pads_u(LAS char* sm, int ch, int half, int lane) {
    const v4u z = {0, 0, 0, 0};
    if (lane < 16) {
        LAS char* ub = sm + L_U + ch * U_CH;
        const int c = 2 * half + (lane >> 3), jx = lane & 7;
        if (jx != 4) *(LAS v4u*)(ub + c * U_CP + (jx < 4 ? 16 * jx : 16 * (252 + jx) + 64)) = z;
    }
}

constexpr int conv_want(int t, int T0, int NA) {
    int wnt = (t + PFA < T0) ? t + PFA : (t + PFB > T0 - 1 ? t + PFB : T0 - 1);
    return t < -1 ? -1 : (wnt < NA - 1 ? wnt : NA - 1);
}
template <int I> struct IC { static constexpr int v = I; };
template <int A, int B, class Fn> __device__ __forceinline__ void static_for(Fn&& fn) {
    if constexpr (A < B) { fn(IC<A>{}); static_for<A + 1, B>(fn); }
}
template <int NTA, int NTB, class Hook>
__device__ __forceinline__ void conv_body(LAS char* ua, LAS char* hb, f32x4& aA0, f32x4& aA1, f32x4& aB0, f32x4& aB1, Hook hook) {
    constexpr int NA = 16 * NTA + 17, NB = 16 * NTB + 17, T0 = NA - NB;
    constexpr int HK = HKV, HK2 = HK2V;
    LAS char* up = ua - 64 * (NA - 1);
    asm volatile("" : "+v"(up), "+v"(hb));
    s16x8 W[NA + 1], BA[NA], BB[NB];
#define HOFF(t) (((t) & 1) ? (((t) + 1) / 2) * 160 : ((t) / 2) * 160 + 64)
    W[0] = *(LAS s16x8*)up;
    constexpr int PF = PFA;
    static_for<0, PF>([&](auto nc) __attribute__((always_inline)) {
        constexpr int nl = decltype(nc)::v;
        W[nl + 1] = *(LAS s16x8*)(up + 64 * (nl + 1));
        BA[nl] = *(LAS s16x8*)(hb + HOFF(nl));
    });
    SCHED();
    static_for<0, NA>([&](auto tc) __attribute__((always_inline)) {
        constexpr int t = decltype(tc)::v, nl = t + PF;
        aA1 = MFMA(W[t], BA[t], aA1);
        SCHED();
        if constexpr (nl + 1 < NA) W[nl + 1] = *(LAS s16x8*)(up + 64 * (nl + 1));
        SCHED();
        if constexpr (t + 1 < NA) aA0 = MFMA(W[t + 1], BA[t], aA0);
        SCHED();
        if constexpr (nl < NA) BA[nl] = *(LAS s16x8*)(hb + HOFF(nl));
        SCHED();
        if constexpr (t >= T0) { aB1 = MFMA(W[t], BB[t - T0], aB1); SCHED(); }
        if constexpr (nl >= T0 && nl < NA) { BB[nl - T0] = *(LAS s16x8*)(hb + HOFF(nl - T0)); SCHED(); }
        if constexpr (t >= T0 && t + 1 < NA) { aB0 = MFMA(W[t + 1], BB[t - T0], aB0); SCHED(); }
        if constexpr (t == HK) { hook(-1); SCHED(); }
    });
}

__global__ __launch_bounds__(512, 2) void fused(const u16* __restrict__ q, const u16* __restrict__ kin, const u16* __restrict__ vin,
                                                const u16* __restrict__ sk, const u16* __restrict__ hk, const u16* __restrict__ Dv,
                                                const u16* __restrict__ w1, const u16* __restrict__ w2, const u16* __restrict__ gw,
                                                char* __restrict__ ws, u16* __restrict__ out) {
    __shared__ __attribute__((aligned(16))) char smem[LDS_BYTES];
    LAS char* sm = (LAS char*)smem;
    const int b = blockIdx.x, x = b & 7, idx = b >> 3;
    const int tid = threadIdx.x, lane = tid & 63;
    const int w = __builtin_amdgcn_readfirstlane(tid >> 6);
    const int j = w & 3;
    const rsrc_t rs = __builtin_amdgcn_make_buffer_rsrc(ws, 0, WS_BYTES, 0x00020000);
    ST_DECL; ST(0);
    unsigned long long pc; unsigned len;
    asm volatile(".Lfused_pc:\n\ts_getpc_b64 %0\n\ts_mov_b32 %1, .Lfused_end-.Lfused_pc" : "=s"(pc), "=s"(len));
    unsigned ctch;
    {
        unsigned co = (unsigned)(idx * 8 + w) * 128u;
        co = co < len ? co : 0u;
        asm volatile("global_load_dword %0, %1, %2" : "=v"(ctch) : "v"(co), "s"(pc) : "memory");
    }
    asm volatile("" :: "s"(q), "s"(kin), "s"(vin), "s"(sk), "s"(hk), "s"(Dv), "s"(w1), "s"(w2), "s"(gw), "s"(ws), "s"(out));
    const int rt = 4 * x + (idx & 3), ct = idx >> 2;
    const int d0 = 64 * x + 2 * idx;
    const int c16 = lane & 15, qd = lane >> 4, qq = c16 >> 2, p4 = lane & 3;
    const bool isG = idx < 28;
    const int rp = 4 * (x >> 1) + (idx & 3), fp = 7 * (x & 1) + (idx >> 2);
    const int ch = j >> 1, half = j & 1;
    const int L0 = 128 * half + lane, L1 = L0 + 64;

#define PLACE(i) (((i) >> 1) * 32768 + j * 8192 + ((i) & 1) * 4096)
    const int w2rd = (4 * qd + qq) * 128 + 32 * ((2 * qd + (qq >> 1)) & 3) + 8 * p4;
#define W2READ(dst, addr)                                                                    \
    do {                                                                                     \
        _Pragma("unroll") for (int n_ = 0; n_ < 4; ++n_) {                                   \
            LAS char* a_ = sm + (addr) + (w2rd ^ (n_ << 5));                                 \
            dst[n_] = cat8(trread(a_), trread(a_ + 2048));                                   \
        }                                                                                    \
    } while (0)
    const unsigned w2off = (unsigned)((lane >> 3) * D + ((((((lane & 7) >> 1) ^ qd) << 1) | (lane & 1)) * 8)) * 2u;
    const u16* w2C = w2 + (size_t)(224 * j) * D + 64 * ct;
#define W2STAGE(base, k_, addr) glds16x4(base + (32 * (k_)) * D, w2off, w2off + 7168u, w2off + 14336u, w2off + 21504u, sm + (addr))
    const int h_off = WS_HF + (rt * 56 + 7 * w) * 4096 + lane * 16;
    const int es = 16 * j + c16, ed = 32 * (w >> 2) + 8 * qd;
    unsigned tag;
    v4u qv;
#define HLOAD(k_) do { _Pragma("unroll") for (int i_ = 0; i_ < 4; ++i_) hbuf[k_][i_] = ld128(rs, h_off + (k_) * 4096 + i_ * 1024); SCHED(); } while (0)
#define G2MMA(wf, k_) do { _Pragma("unroll") for (int n_ = 0; n_ < 4; ++n_) _Pragma("unroll") for (int s_ = 0; s_ < 4; ++s_) \
        acc2[n_][s_] = MFMA(wf[n_], __builtin_bit_cast(s16x8, hbuf[k_][s_]), acc2[n_][s_]); SCHED(); } while (0)
#define LPIECE(i) ((i) == 15 ? PLACE(4) + 2048 : L_CV + j * 16384 + (i) * 1024)

    unsigned ev = 0, dvr = 0, tch = 0;
    LAS char* kvw = sm + L_KV + j * 5632;
    if (w < 4) {
        const int r_ = 64 * idx + 16 * j - 8 + (lane >> 3);
        const unsigned co_ = (unsigned)(64 * x + (lane & 7) * 8) * 2u;
        glds16(kin, (unsigned)((r_ < 0 ? 0 : r_) * D) * 2u + co_, kvw);
        const unsigned o1_ = (unsigned)((r_ + 8) * D) * 2u + co_;
        glds16(kin, o1_, kvw + 1024);
        glds16(vin, o1_, kvw + 3072);
        if (lane < 32) glds16(sk, (unsigned)(256 * x + lane * 8) * 2u, kvw + 5120);
        glds16(kin + 8 * D, o1_, kvw + 2048);
        glds16(vin + 8 * D, o1_, kvw + 4096);
        asm volatile("global_load_dword %0, %2, %3 sc1\n\tglobal_load_ushort %1, %2, %4" : "=&v"(ev), "=&v"(dvr) : "v"(0), "s"(ws + WS_E), "s"(Dv + d0 + ch) : "memory");
        const int fpc = fp < 14 ? fp : 13, l5 = (lane & 31) + 32 * (j & 1);
        const int id = 128 + fpc * 64 + l5;
        const unsigned to = j < 2 ? (unsigned)((rp * 128 + (id & 127)) * 1024 + (id >> 7) * 128) : (unsigned)((32 * rp + (l5 >> 1)) * (F * 2) + fpc * 256 + (l5 & 1) * 128);
        asm volatile("global_load_dword %0, %1, %2" : "=v"(tch) : "v"(to), "s"(j < 2 ? (const char*)q : (const char*)w1) : "memory");
    }
    SCHED();

    if (w < 4) {
        unsigned dvs;
        if (w == 0 && lane < 32 && lane != F_TAG) *(LAS unsigned*)(sm + L_FL + 4 * lane) = 0u;
        const int sh = j & 1, fh = j >> 1;
        LAS char* bad[2][2]; LAS char* aad[2][4];
        {
            const int key = qq | ((qd & 1) << 2);
#pragma unroll
            for (int sp = 0; sp < 2; ++sp) {
                bad[sp][0] = sm + sp * 65536 + (64 * sh + c16) * 128 + 16 * (qd ^ (c16 >> 1));
                bad[sp][1] = sm + sp * 65536 + (64 * sh + c16) * 128 + 16 * ((4 + qd) ^ (c16 >> 1));
#pragma unroll
                for (int ft = 0; ft < 4; ++ft) aad[sp][ft] = sm + sp * 65536 + 16384 + (8 * qd + qq) * 256 + 32 * ((4 * fh + ft) ^ key) + 8 * p4;
                asm volatile("" : "+v"(bad[sp][0]), "+v"(bad[sp][1]), "+v"(aad[sp][0]), "+v"(aad[sp][1]), "+v"(aad[sp][2]), "+v"(aad[sp][3]));
            }
        }
        LAS char* tsrc = sm + L_T + (16 * j + (lane >> 3)) * 144 + (lane & 7) * 16;
        int tdst = WS_UT + (64 * x + 16 * j + (lane >> 3)) * 4096 + (1984 - 64 * idx) * 2 + (lane & 7) * 16;
        asm volatile("" : "+v"(tsrc), "+v"(tdst));
        WAIT_V(5);
        ST(1);
        *(LAS v4u*)(sm + L_T + lane * 144 + (7 - 2 * j) * 16) = kv_prep(kvw, 0, lane, idx == 0 && j == 0);
        SCHED(); WAIT_V(3);
        *(LAS v4u*)(sm + L_T + lane * 144 + (6 - 2 * j) * 16) = kv_prep(kvw, 1, lane, false);
        WAIT_L0(); SCHED();
        ST(2);
        BAR();
#define UT_STORE() do { v4u o0_ = *(LAS v4u*)tsrc, o1_ = *(LAS v4u*)(tsrc + 8 * 144); st128(o0_, rs, tdst); st128(o1_, rs, tdst + 8 * 4096); } while (0)
#define GET_TAG() do { \
            asm volatile("s_waitcnt vmcnt(0)\n\tv_readfirstlane_b32 %0, %2\n\tv_readfirstlane_b32 %1, %3\n\ts_nop 3" : "=s"(tag), "=s"(dvs) : "v"(ev), "v"(dvr), "v"(tch), "v"(ctch) : "memory"); \
            tag += 1u; \
            if (tid == 0) *(LAS unsigned*)(sm + L_FL + 4 * F_TAG) = tag; } while (0)
        const int fu_st = WS_FU + (x * 32 + idx) * 128;
        if (isG) {
            f32x4 acc[4][4] = {};
            s16x8 fa[2][2][4], fb[2][2][4];
#define READ(c)                                                                                     \
    do {                                                                                            \
        _Pragma("unroll") for (int kk = 0; kk < 2; ++kk) {                                          \
            _Pragma("unroll") for (int ft = 0; ft < 4; ++ft) {                                      \
                fb[(c) & 1][kk][ft] = *(LAS s16x8*)(bad[((c) >> 1) & 1][kk] + ((c) & 1) * 32768 + 2048 * ft); \
                LAS char* a_ = aad[((c) >> 1) & 1][ft] + ((c) & 1) * 32768 + kk * 8192;             \
                fa[(c) & 1][kk][ft] = cat8(trread(a_), trread(a_ + 1024));                          \
            }                                                                                       \
        }                                                                                           \
    } while (0)
#define MMA(c)                                                                                      \
    do {                                                                                            \
        _Pragma("unroll") for (int kk = 0; kk < 2; ++kk)                                            \
            _Pragma("unroll") for (int ft = 0; ft < 4; ++ft)                                        \
                _Pragma("unroll") for (int st = 0; st < 4; ++st)                                    \
                    acc[ft][st] = MFMA(fa[(c) & 1][kk][ft], fb[(c) & 1][kk][st], acc[ft][st]);      \
    } while (0)
#define ILV() do { _Pragma("unroll") for (int g_ = 0; g_ < 24; ++g_) { __builtin_amdgcn_sched_group_barrier(0x100, 1, 0); __builtin_amdgcn_sched_group_barrier(0x8, 1, 0); } \
                   __builtin_amdgcn_sched_group_barrier(0x8, 8, 0); SCHED(); } while (0)
            STF(1); SCHED(); UT_STORE(); READ(0); SCHED(); WAIT_L0(); STF(2);
            BAR(); STF(3); SCHED(); READ(1); MMA(0); ILV(); WAIT_L0(); STF(4);
            BAR(); STF(5); SCHED(); READ(2); MMA(1); ILV(); STF(6);
            GET_TAG(); WAIT_L0(); STF(7);
            BAR(); STF(8);
            if (tid == 0) st32(tag, rs, fu_st);
            SCHED(); READ(3); MMA(2); ILV(); WAIT_L0(); STF(9);
            ST(3);
            BAR(); STF(10); SCHED(); READ(4); MMA(3); ILV(); WAIT_L0(); STF(11);
            BAR(); STF(12); SCHED(); READ(5); MMA(4); ILV(); WAIT_L0(); STF(13);
            BAR(); STF(14); SCHED(); READ(6); MMA(5); ILV(); WAIT_L0();
            BAR(); STF(15); SCHED();
            ST(4);
            READ(7); MMA(6); ILV(); WAIT_L0();
            lds_set(sm, F_C7 + j, lane);
            SCHED();
            MMA(7);
            ST(5);
            const int hst = WS_HF + ((2 * rp + sh) * 56 + 4 * fp + 2 * fh) * 4096 + lane * 16;
#pragma unroll
            for (int pp = 0; pp < 2; ++pp)
#pragma unroll
                for (int st = 0; st < 4; ++st) st128(silu8(acc[2 * pp][st], acc[2 * pp + 1][st]), rs, hst + pp * 4096 + st * 1024);
            ST(6);
        } else {
            UT_STORE();
            const int r0 = 512 * (x >> 1) + 64 * (4 * (x & 1) + (idx - 28)) + 16 * j;
            v4u gv = *(const GAS v4u*)(gw + lane * 8);
            float gs[2];
#pragma unroll 1
            for (int hh = 0; hh < 2; ++hh) {
                v4u qr[8];
#pragma unroll
                for (int i = 0; i < 8; ++i) qr[i] = *(const GAS v4u*)(q + (size_t)(r0 + 8 * hh + i) * D + lane * 8);
                if (hh == 0) {
                    GET_TAG(); WAIT_L0();
                    BAR();
                    if (tid == 0) st32(tag, rs, fu_st);
                }
                float s[8];
#pragma unroll
                for (int i = 0; i < 8; ++i) {
                    s[i] = 0.f;
#pragma unroll
                    for (int e = 0; e < 4; ++e) {
                        unsigned r = pk2(bflo(qr[i][e]) * bflo(gv[e]), bfhi(qr[i][e]) * bfhi(gv[e]));
                        s[i] += bflo(r) + bfhi(r);
                    }
                }
#pragma unroll
                for (int i = 0; i < 4; ++i) { const bool hi = lane & 32; float snd = hi ? s[i] : s[i + 4], kp = hi ? s[i + 4] : s[i]; s[i] = kp + __shfl_xor(snd, 32); }
#pragma unroll
                for (int i = 0; i < 2; ++i) { const bool hi = lane & 16; float snd = hi ? s[i] : s[i + 2], kp = hi ? s[i + 2] : s[i]; s[i] = kp + __shfl_xor(snd, 16); }
                { const bool hi = lane & 8; float snd = hi ? s[0] : s[1], kp = hi ? s[1] : s[0]; s[0] = kp + __shfl_xor(snd, 8); }
                s[0] += __shfl_xor(s[0], 4); s[0] += __shfl_xor(s[0], 2); s[0] += __shfl_xor(s[0], 1);
                if (hh == 0) gs[0] = s[0]; else gs[1] = s[0];
            }
            if ((lane & 7) < 2) {
                float t = rbf((lane & 1) ? gs[1] : gs[0]);
                float g = rbf(__builtin_amdgcn_rcpf(1.0f + __builtin_amdgcn_exp2f(-1.44269504088896f * t)));
                st64((v2u){__float_as_uint(g), tag}, rs, WS_G + (r0 + 8 * (lane & 1) + (lane >> 3)) * 8);
            }
        }
#define H_PUBLISH() do { WAIT_V(0); if (isG && lane == 0) st32(tag, rs, WS_FH + (rp * 16 + fp) * 128 + j * 4); ST(8); } while (0)
        s16x8 wl[7][4];
        lds_wait(sm, F_C01 + j, 0, lane);
        W2READ(wl[0], PLACE(0)); W2READ(wl[1], PLACE(1));
        WAIT_L0(); SCHED();
        lds_set(sm, F_PART + j, lane);
        H_PUBLISH();
        lds_wait(sm, F_FILL + 2 * ch, 1, lane);
        CBAR();
        ST(7);
        f32x4 aA0 = {}, aA1 = {}, aB0 = {}, aB1 = {};
        const int b0 = c16 >> 3, b1 = c16 & 7;
        v2u uu[2][2];
        {
            LAS char* u3 = sm + L_U + ch * U_CH + 3 * U_CP + 64 + 2 * (2048 - (8 * qd + 4 * b0 + 64 * b1));
#pragma unroll
            for (int t = 0; t < 2; ++t)
#pragma unroll
                for (int mt = 0; mt < 2; ++mt) uu[t][mt] = *(LAS v2u*)(u3 - 1024 * (t ? half : 3 - half) - 64 * mt);
        }
        {
            LAS char* ua_ = sm + L_U + ch * U_CH + (c16 & 3) * U_CP + 64 + 4096 + 16 * (qd - (c16 >> 2));
            LAS char* hb_ = sm + L_H + ch * H_CH + (c16 >> 3) * H_CP + (c16 & 7) * 160 + qd * 16;
            auto hook = [&](int i) __attribute__((always_inline)) {};
            if (half == 0) conv_body<3, 0>(ua_, hb_, aA0, aA1, aB0, aB1, hook);
            else conv_body<2, 1>(ua_, hb_, aA0, aA1, aB0, aB1, hook);
        }
        ST(9);
        lds_set(sm, F_CONV + j, lane);
        {
            const float dd = bflo(dvs);
            const int srcl = ((lane >> 3) + 8 * (lane & 1) + 16 * ((lane >> 1) & 3)) * 4;
            const int zb = WS_Z + (d0 + ch) * 8192 + (lane >> 3) * 256 + (lane & 7) * 16;
#pragma unroll
            for (int t = 0; t < 2; ++t)
#pragma unroll
                for (int mt = 0; mt < 2; ++mt) {
                    f32x4 y = t ? (mt ? aB1 : aB0) : (mt ? aA1 : aA0);
                    v2u u_ = uu[t][mt];
                    unsigned y01 = pk2(y[0], y[1]), y23 = pk2(y[2], y[3]);
                    unsigned p01 = pk2(bfhi(u_.y) * dd, bflo(u_.y) * dd), p23 = pk2(bfhi(u_.x) * dd, bflo(u_.x) * dd);
                    unsigned z0 = pk2(bflo(y01) + bflo(p01), bfhi(y01) + bfhi(p01));
                    unsigned z1 = pk2(bflo(y23) + bflo(p23), bfhi(y23) + bfhi(p23));
                    z0 = __builtin_amdgcn_ds_bpermute(srcl, z0);
                    z1 = __builtin_amdgcn_ds_bpermute(srcl, z1);
                    st128((v4u){z0, tag, z1, tag}, rs, zb + (t ? half : 3 - half) * 2048 + mt * 128);
                }
            SCHED();
        }
        ST(10);
        lds_wait(sm, F_W2OK + j, 0, lane);
        ST(11);
        v4u hbuf[7][4];
        f32x4 acc2[4][4] = {};
        HLOAD(0); HLOAD(1); HLOAD(2); HLOAD(3);
        if (!CALL) G2MMA(wl[0], 0);
        HLOAD(4); HLOAD(5); HLOAD(6);
        qv = *(const GAS v4u*)(q + (size_t)(64 * rt + es) * D + 64 * ct + ed);
        lds_set(sm, F_C7 + j, lane, 2u);
        SCHED();
        if (CALL) G2MMA(wl[0], 0);
        W2READ(wl[2], PLACE(2)); SCHED(); G2MMA(wl[1], 1);
        W2READ(wl[3], PLACE(3)); SCHED(); G2MMA(wl[2], 2);
        W2READ(wl[4], PLACE(5)); SCHED(); G2MMA(wl[3], 3);
        W2READ(wl[5], PLACE(0)); SCHED(); G2MMA(wl[4], 4);
        W2READ(wl[6], PLACE(1)); SCHED(); G2MMA(wl[5], 5);
        G2MMA(wl[6], 6);
        ST(12);
#pragma unroll
        for (int s = 0; s < 4; ++s) {
            LAS char* pp = sm + PLACE(s) + c16 * 256;
#pragma unroll
            for (int n = 0; n < 4; ++n) *(LAS f32x4*)(pp + (((4 * n + qd) ^ c16) << 4)) = acc2[n][s];
        }
    } else {
        const int fu_off = WS_FU + (x * 32 + (lane & 31)) * 128;
        const u16* w2L = w2 + (size_t)(224 * w) * D + 64 * ct;
        const unsigned xs0 = (unsigned)((rp * 128 + 32 * j + (lane >> 3)) * D + (((lane & 7) ^ qd) * 8)) * 2u;
        const unsigned xs1 = (unsigned)((rp * 128 + 32 * j + (lane >> 3)) * D + (((lane & 7) ^ (4 + qd)) * 8)) * 2u + 7168u;
        const unsigned ws0 = (unsigned)((16 * j + qd) * F + fp * 128 + (((((c16 >> 1) ^ qd) << 1) | (lane & 1)) * 8)) * 2u;
        const unsigned ws1 = (unsigned)((16 * j + qd) * F + fp * 128 + (((((c16 >> 1) ^ (qd | 4)) << 1) | (lane & 1)) * 8)) * 2u + 2u * 13312u;
#define STAGE(c)                                                                                   \
    do {                                                                                           \
        LAS char* sl = sm + ((c) & 3) * 32768 + 4096 * j;                                          \
        glds16x4(q + (c) * 64, xs0, xs1, xs0 + 14336u, xs1 + 14336u, sl);                          \
        glds16x4(w1 + (c) * 64 * F, ws0, ws0 + 13312u, ws1, ws1 + 13312u, sl + 16384);             \
    } while (0)
        const char* hrow = (const char*)hk + (size_t)(d0 + ch) * 4096;
        const int urow = WS_UT + (d0 + ch) * 4096;
        v4u ha0, ha1, ub0, ub1; v2u hb0, hb1, ua0, ua1;
#define HKLOAD() asm volatile("global_load_dwordx4 %0, %4, %6" NTH "\n\tglobal_load_dwordx2 %1, %4, %6 offset:16" NTH "\n\t" \
                     "global_load_dwordx4 %2, %4, %6 offset:1024" NTH "\n\tglobal_load_dwordx2 %3, %5, %6" NTH \
                     : "=&v"(ha0), "=&v"(hb0), "=&v"(ha1), "=&v"(hb1) \
                     : "v"(16 * L0), "v"(L1 == 255 ? 0 : 16 * L1 + 16), "s"(hrow) : "memory")
        if (isG) {
            STAGE(0); STAGE(1); STAGE(2);
            ST(1);
            asm volatile("s_waitcnt vmcnt(16)" :: "v"(ctch) : "memory"); BAR(); STAGE(3);
            ST(2);
            WAIT_V(16); BAR(); STAGE(4);
            WAIT_V(16); BAR(); STAGE(5);
            WAIT_V(16); BAR(); STAGE(6);
            ST(3);
            WAIT_V(16); BAR(); STAGE(7);
        } else {
            unsigned t0_, t1_, t2_, t3_;
            const unsigned wo = (unsigned)((((idx - 28) * 8 + x) * 4 + j) * 112 + lane) * 128u;
            asm volatile("global_load_dword %0, %2, %4 sc1\n\tglobal_load_dword %1, %3, %5 sc1"
                         : "=&v"(t0_), "=&v"(t1_) : "v"((unsigned)lane << 18), "v"((unsigned)(lane & 7) << 18), "s"(ws), "s"(out) : "memory");
            BAR();
            asm volatile("global_load_dword %0, %2, %4\n\tglobal_load_dword %1, %3, %4" : "=&v"(t2_), "=&v"(t3_) : "v"(wo), "v"(wo + (lane < 48 ? 8192u : 0u)), "s"(w2) : "memory");
            BAR();
            asm volatile("s_waitcnt vmcnt(0)" :: "v"(t0_), "v"(t1_), "v"(t2_), "v"(t3_), "v"(ctch) : "memory");
            __builtin_amdgcn_s_sleep(20);
        }
        HKLOAD();
        tag = __builtin_amdgcn_readfirstlane(*(volatile LAS unsigned*)(sm + L_FL + 4 * F_TAG));
        if (isG) { WAIT_V(20); BAR(); }
        unsigned pr = 0;
        if (j == 0) asm volatile("global_load_dword %0, %1, %2 sc1" : "=v"(pr) : "v"(fu_off), "s"(ws) : "memory");
        if (isG) { WAIT_V(12); BAR(); }
        W2STAGE(w2C, 0, PLACE(0)); W2STAGE(w2C, 1, PLACE(1));
        asm volatile("s_waitcnt vmcnt(8)" : "+v"(pr), "+v"(ha0), "+v"(hb0), "+v"(ha1), "+v"(hb1) :: "memory");
        const bool utok = j != 0 || __all(pr == tag);
        if (j == 0 && utok) lds_set(sm, F_UT, lane);
        if (isG) { WAIT_L0(); BAR(); }
        ST(4);
        if (!utok) { poll_flags(rs, fu_off, tag); lds_set(sm, F_UT, lane); }
        lds_wait(sm, F_UT, 0, lane);
        ST(8);
        asm volatile("global_load_dwordx2 %0, %4, %6 sc1\n\tglobal_load_dwordx4 %1, %5, %6 sc1\n\t"
                     "global_load_dwordx2 %2, %5, %6 offset:1016 sc1\n\tglobal_load_dwordx4 %3, %5, %6 offset:1024 sc1"
                     : "=&v"(ua0), "=&v"(ub0), "=&v"(ua1), "=&v"(ub1)
                     : "v"(urow + (L0 == 0 ? 0 : 16 * L0 - 8)), "v"(urow + 16 * L0), "s"(ws) : "memory");
        W2STAGE(w2L, 0, PLACE(2)); W2STAGE(w2L, 1, PLACE(3));
        __builtin_amdgcn_s_setprio(2);
        fill_pads_h(sm, ch, half, lane);
        fill_h(sm, ha0, hb0, ch, L0); fill_h(sm, ha1, hb1, ch, L1);
        SCHED();
        __builtin_amdgcn_s_setprio(0);
        W2STAGE(w2L, 2, PLACE(4)); W2STAGE(w2L, 3, PLACE(5));
        __builtin_amdgcn_s_setprio(2);
        if (isG) lds_wait(sm, F_C7, 3, lane);
        asm volatile("s_waitcnt vmcnt(16)" : "+v"(ua0), "+v"(ub0), "+v"(ua1), "+v"(ub1) :: "memory");
        lds_set(sm, F_C01 + j, lane);
        ST(9);
        fill_pads_u(sm, ch, half, lane);
        fill_u(sm, ua0, ub0, ch, L0); fill_u(sm, ua1, ub1, ch, L1);
        WAIT_L0(); SCHED();
        lds_set(sm, F_FILL + j, lane);
        __builtin_amdgcn_s_setprio(0);
        ST(5);
        s16x8 wr[6][4];
        WAIT_V(8);
        W2READ(wr[0], PLACE(2)); W2READ(wr[1], PLACE(3));
        WAIT_L0(); SCHED();
        W2STAGE(w2L, 4, PLACE(2)); W2STAGE(w2L, 5, PLACE(3));
        WAIT_V(8);
        W2READ(wr[2], PLACE(4)); W2READ(wr[3], PLACE(5));
        WAIT_L0(); SCHED();
        W2STAGE(w2L, 6, PLACE(4)); W2STAGE(w2C, 4, PLACE(5));
        ST(6);
        lds_wait(sm, F_PART + j, 0, lane);
        W2STAGE(w2C, 5, PLACE(0)); W2STAGE(w2C, 6, PLACE(1));
        WAIT_V(16);
        W2READ(wr[4], PLACE(2)); W2READ(wr[5], PLACE(3));
        WAIT_L0(); SCHED();
        W2STAGE(w2C, 2, PLACE(2)); W2STAGE(w2C, 3, PLACE(3));
        ST(7);
        {
            const int kpl = ((7 * (w - 4 * ((lane >> 2) & 1))) >> 1) + (lane & 3);
            poll_flags(rs, WS_FH + ((rt >> 1) * 16 + (kpl >> 1)) * 128 + ((rt & 1) + 2 * (kpl & 1)) * 4, tag);
        }
        ST(11);
        v4u hbuf[7][4];
        f32x4 acc2[4][4] = {};
        lds_set(sm, F_W2OK + j, lane);
        __builtin_amdgcn_s_setprio(LPRIO);
        HLOAD(0); HLOAD(1); HLOAD(2); HLOAD(3);
        G2MMA(wr[0], 0); HLOAD(4); HLOAD(5);
        G2MMA(wr[1], 1); HLOAD(6);
        qv = *(const GAS v4u*)(q + (size_t)(64 * rt + es) * D + 64 * ct + ed);
        SCHED();
        s16x8 wl[4];
        G2MMA(wr[2], 2);
        G2MMA(wr[3], 3);
        G2MMA(wr[4], 4);
        W2READ(wl, PLACE(4)); SCHED(); G2MMA(wr[5], 5);
        G2MMA(wl, 6);
        ST(12);
        __builtin_amdgcn_s_setprio(0);
        lds_wait(sm, F_CONV, 3, lane);
#pragma unroll
        for (int s = 0; s < 4; ++s) {
            LAS char* pp = sm + (s < 3 ? L_CV + j * 16384 + s * 4096 : PLACE(4)) + c16 * 256;
#pragma unroll
            for (int n = 0; n < 4; ++n) *(LAS f32x4*)(pp + (((4 * n + qd) ^ c16) << 4)) = acc2[n][s];
        }
        SCHED();
        lds_wait(sm, F_C7 + j, 0, lane, 2u);
        const int zoff = WS_Z + (64 * ct + 16 * j + qd) * 8192 + rt * 256 + c16 * 16;
        v4u z[4]; v2u gr;
#pragma unroll 1
        for (unsigned spins = 0; spins < SPIN_CAP; ++spins) {
            CBAR();
#pragma unroll
            for (int i = 0; i < 4; ++i) z[i] = ld128(rs, zoff + i * 4 * 8192);
            gr = ld64(rs, WS_G + (64 * rt + lane) * 8);
            bool ok = gr.y == tag;
#pragma unroll
            for (int i = 0; i < 4; ++i) ok = ok && z[i].y == tag && z[i].w == tag;
            if (__all(ok)) break;
            __builtin_amdgcn_s_sleep(ZSLP);
        }
#pragma unroll
        for (int i = 0; i < 4; ++i) *(LAS v2u*)(sm + L_CV + j * 16384 + 12288 + (4 * i + qd) * 128 + c16 * 8) = (v2u){z[i].x, z[i].z};
        if (j == 0) *(LAS unsigned*)(sm + L_CV + 12288 + 2048 + lane * 4) = gr.x;
        if (b == 255 && w == 5) {
#pragma unroll 1
            for (unsigned spins = 0; spins < SPIN_CAP; ++spins) {
                CBAR();
                bool ok = true;
#pragma unroll
                for (int i = 0; i < 4; ++i) ok = (ld32(rs, WS_FU + (i * 64 + lane) * 128) == tag) && ok;
                if (__all(ok)) break;
                __builtin_amdgcn_s_sleep(2);
            }
            if (lane == 0) st32(tag, rs, WS_E);
        }
    }
    WAIT_L0();
    ST(13);
    BAR();
    {
        const int zr = ed + qq;
        LAS char* zp = sm + L_CV + 12288 + (zr >> 4) * 16384 + (zr & 15) * 128 + 32 * j + 8 * p4;
        v2u za = __builtin_bit_cast(v2u, trread(zp)), zb = __builtin_bit_cast(v2u, trread(zp + 512));
        const float gg = *(LAS float*)(sm + L_CV + 12288 + 2048 + es * 4);
        const int sl0 = ((ed >> 2) ^ c16) << 4, sl1 = (((ed >> 2) + 1) ^ c16) << 4;
        LAS char* pp = sm + (j >> 1) * 32768 + (j & 1) * 4096 + c16 * 256;
        LAS char* pl = sm + (j < 3 ? L_CV + j * 4096 : 65536) + c16 * 256;
        const int lstr = j < 3 ? 16384 : 8192;
        f32x4 s0 = *(LAS f32x4*)(pp + sl0), s1 = *(LAS f32x4*)(pp + sl1);
#pragma unroll
        for (int i = 1; i < 4; ++i) { s0 += *(LAS f32x4*)(pp + i * 8192 + sl0); s1 += *(LAS f32x4*)(pp + i * 8192 + sl1); }
#pragma unroll
        for (int i = 0; i < 4; ++i) { s0 += *(LAS f32x4*)(pl + i * lstr + sl0); s1 += *(LAS f32x4*)(pl + i * lstr + sl1); }
        const float g1 = rbf(1.0f - gg);
        unsigned zw[4] = {za.x, za.y, zb.x, zb.y};
        float bs[8] = {s0[0], s0[1], s0[2], s0[3], s1[0], s1[1], s1[2], s1[3]};
        v4u o;
#pragma unroll
        for (int e = 0; e < 4; ++e) {
            unsigned bb = pk2(bs[2 * e], bs[2 * e + 1]);
            unsigned aa = pk2(bflo(qv[e]) * bflo(zw[e]), bfhi(qv[e]) * bfhi(zw[e]));
            unsigned ga = pk2(gg * bflo(aa), gg * bfhi(aa));
            unsigned gb = pk2(g1 * bflo(bb), g1 * bfhi(bb));
            o[e] = pk2(bflo(ga) + bflo(gb), bfhi(ga) + bfhi(gb));
        }
        *(GAS v4u*)(out + (size_t)(64 * rt + es) * D + 64 * ct + ed) = o;
    }
    asm volatile(".Lfused_end:" ::: "memory");
    ST(14);
    ST(15);
    ST_END(isG ? 0 : 1);
}

extern "C" void kernel_launch(void* const* d_in, const int* in_sizes, int n_in, void* d_out, int out_size, void* d_ws, size_t ws_size,
                              hipStream_t stream) {
    const u16* q = (const u16*)d_in[0]; const u16* k = (const u16*)d_in[1]; const u16* v = (const u16*)d_in[2];
    const u16* sk = (const u16*)d_in[3]; const u16* hk = (const u16*)d_in[4]; const u16* Dv = (const u16*)d_in[5];
    const u16* w1 = (const u16*)d_in[7]; const u16* w2 = (const u16*)d_in[8]; const u16* gw = (const u16*)d_in[9];
    fused<<<256, 512, 0, stream>>>(q, k, v, sk, hk, Dv, w1, w2, gw, (char*)d_ws, (u16*)d_out);
}
```

```cpp
#include <hip/hip_runtime.h>
#include <hip/hip_bf16.h>
#include <stdint.h>

#define LAS __attribute__((address_space(3)))
#define GAS __attribute__((address_space(1)))
typedef unsigned short u16;
typedef short s16x4 __attribute__((ext_vector_type(4)));
typedef short s16x8 __attribute__((ext_vector_type(8)));
typedef float f32x4 __attribute__((ext_vector_type(4)));
typedef unsigned v4u __attribute__((ext_vector_type(4)));
typedef unsigned v2u __attribute__((ext_vector_type(2)));
typedef __bf16 bf16x2_t __attribute__((ext_vector_type(2)));
typedef float f32x2 __attribute__((ext_vector_type(2)));
typedef __amdgpu_buffer_rsrc_t rsrc_t;

constexpr int S = 2048, D = 512, F = 1792;

constexpr int WS_HF = 0;
constexpr int WS_UT = 8 << 20;
constexpr int WS_Z = 10 << 20;
constexpr int WS_G = 14 << 20;
constexpr int WS_FU = WS_G + 65536;
constexpr int WS_FH = WS_FU + 32768;
constexpr int WS_FZ = WS_FH + 32768;
constexpr int WS_E = WS_FZ + 32768;
constexpr int WS_BYTES = 16 << 20;
constexpr int SC1 = 16;
constexpr unsigned SPIN_CAP = 1u << 16;

__device__ __forceinline__ float bflo(unsigned w) { return __uint_as_float(w << 16); }
__device__ __forceinline__ float bfhi(unsigned w) { return __uint_as_float(w & 0xFFFF0000u); }
__device__ __forceinline__ unsigned pk2v(f32x2 f) { return __builtin_bit_cast(unsigned, __builtin_convertvector(f, bf16x2_t)); }
__device__ __forceinline__ unsigned pk2(float lo, float hi) { return pk2v((f32x2){lo, hi}); }
__device__ __forceinline__ float rbf(float f) { return bflo(pk2(f, f)); }
__device__ __forceinline__ s16x4 trread(LAS char* p) { return __builtin_amdgcn_ds_read_tr16_b64_v4i16((LAS s16x4*)p); }
__device__ __forceinline__ s16x8 cat8(s16x4 a, s16x4 b) { return __builtin_shufflevector(a, b, 0, 1, 2, 3, 4, 5, 6, 7); }
__device__ __forceinline__ void glds16(const void* base, unsigned voff, LAS char* l) {
    asm volatile("s_mov_b32 m0, %2\n\ts_nop 0\n\tglobal_load_lds_dwordx4 %0, %1 nt"
                 :: "v"(voff), "s"(base), "s"((unsigned)(size_t)l) : "memory");
}
__device__ __forceinline__ void glds16x4(const void* base, unsigned vo0, unsigned vo1, unsigned vo2, unsigned vo3, LAS char* l) {
    asm volatile("s_mov_b32 m0, %5\n\ts_nop 0\n\tglobal_load_lds_dwordx4 %0, %4\n\tglobal_load_lds_dwordx4 %1, %4 offset:1024\n\t"
                 "global_load_lds_dwordx4 %2, %4 offset:2048\n\tglobal_load_lds_dwordx4 %3, %4 offset:3072"
                 :: "v"(vo0), "v"(vo1), "v"(vo2), "v"(vo3), "s"(base), "s"((unsigned)(size_t)l) : "memory");
}
#define WAIT_V(n) asm volatile("s_waitcnt vmcnt(" #n ")" ::: "memory")
#define WAIT_L0() __builtin_amdgcn_s_waitcnt(0xC07F)
#define BAR() asm volatile("s_barrier" ::: "memory")
#define SCHED() __builtin_amdgcn_sched_barrier(0)
#define CBAR() asm volatile("" ::: "memory")
#define MFMA(a, b, c) __builtin_amdgcn_mfma_f32_16x16x32_bf16(a, b, c, 0, 0, 0)

__device__ __forceinline__ v4u ld128(rsrc_t rs, int off) { return __builtin_amdgcn_raw_buffer_load_b128(rs, off, 0, SC1); }
__device__ __forceinline__ v2u ld64(rsrc_t rs, int off) { return __builtin_amdgcn_raw_buffer_load_b64(rs, off, 0, SC1); }
__device__ __forceinline__ unsigned ld32(rsrc_t rs, int off) { return __builtin_amdgcn_raw_buffer_load_b32(rs, off, 0, SC1); }
__device__ __forceinline__ void st128(v4u v, rsrc_t rs, int off) { __builtin_amdgcn_raw_buffer_store_b128(v, rs, off, 0, SC1); }
__device__ __forceinline__ void st64(v2u v, rsrc_t rs, int off) { __builtin_amdgcn_raw_buffer_store_b64(v, rs, off, 0, SC1); }
__device__ __forceinline__ void st32(unsigned v, rsrc_t rs, int off) { __builtin_amdgcn_raw_buffer_store_b32(v, rs, off, 0, SC1); }
__device__ __forceinline__ void poll_flags(rsrc_t rs, int off, unsigned tag) {
#pragma unroll 1
    for (unsigned spins = 0; spins < SPIN_CAP; ++spins) {
        CBAR();
        unsigned v = ld32(rs, off);
        if (__all(v == tag)) break;
        __builtin_amdgcn_s_sleep(4);
    }
    CBAR();
}
__device__ __forceinline__ void poll_flags_at(char* ws, int off, unsigned tag, bool active) {
#pragma unroll 1
    for (unsigned spins = 0; spins < SPIN_CAP; ++spins) {
        CBAR();
        unsigned v = tag;
        if (active) asm volatile("global_atomic_or %0, %1, %2, %3 sc0 sc1\n\ts_waitcnt vmcnt(0)" : "=&v"(v) : "v"(off), "v"(0), "s"(ws) : "memory");
        active = v != tag;
        if (!__any(active)) break;
        __builtin_amdgcn_s_sleep(4);
    }
    CBAR();
}
__device__ __forceinline__ v4u silu8(f32x4 a0, f32x4 a1) {
    f32x2 x[4] = {{a0[0], a0[1]}, {a0[2], a0[3]}, {a1[0], a1[1]}, {a1[2], a1[3]}};
    f32x2 t[4];
#pragma unroll
    for (int i = 0; i < 4; ++i) t[i] = x[i] * -1.44269504088896f;
#pragma unroll
    for (int i = 0; i < 4; ++i) { t[i].x = __builtin_amdgcn_exp2f(t[i].x); t[i].y = __builtin_amdgcn_exp2f(t[i].y); }
#pragma unroll
    for (int i = 0; i < 4; ++i) t[i] = t[i] + 1.0f;
#pragma unroll
    for (int i = 0; i < 4; ++i) { t[i].x = __builtin_amdgcn_rcpf(t[i].x); t[i].y = __builtin_amdgcn_rcpf(t[i].y); }
    v4u o;
#pragma unroll
    for (int i = 0; i < 4; ++i) o[i] = pk2v(x[i] * t[i]);
    return o;
}

constexpr int L_CV = 98304;
constexpr int L_KV = 131072;
constexpr int L_T = L_KV + 22528;
constexpr int U_CP = 4416, U_CH = 4 * U_CP, H_CP = 6400, H_CH = 2 * H_CP;
constexpr int L_U = L_CV, L_H = L_CV + 2 * U_CH;
constexpr int LDS_BYTES = 163840;
constexpr int L_FL = LDS_BYTES - 128;
enum { F_W2OK = 0, F_C7 = 4, F_FILL = 8, F_CONV = 12, F_C01 = 16, F_PART = 20, F_TAG = 24, F_UT = 25 };

__device__ __forceinline__ void lds_set(LAS char* sm, int i, int lane, unsigned v = 1u) {
    if (lane == 0) *(volatile LAS unsigned*)(sm + L_FL + 4 * i) = v;
}
__device__ __forceinline__ void lds_wait(LAS char* sm, int i0, int mask, int lane, unsigned need = 1u) {
#pragma unroll 1
    for (unsigned spins = 0; spins < (1u << 22); ++spins) {
        unsigned v = *(volatile LAS unsigned*)(sm + L_FL + 4 * (i0 + (lane & mask)));
        if (__all(v >= need)) break;
        __builtin_amdgcn_s_sleep(1);
    }
    CBAR();
}

__device__ __forceinline__ v4u kv_prep(LAS char* kvw, int e, int lane, bool zero_halo) {
    const int gq = lane >> 4, qq = (lane & 15) >> 2, p = lane & 3;
    LAS char* kb = kvw + (8 * e + 4 + qq) * 128 + 32 * gq + 8 * p;
    v2u k0 = __builtin_bit_cast(v2u, trread(kb));
    v2u k1 = __builtin_bit_cast(v2u, trread(kb + 512));
    v2u k2 = __builtin_bit_cast(v2u, trread(kb + 1024));
    LAS char* vb = kvw + 3072 + (8 * e + qq) * 128 + 32 * gq + 8 * p;
    v2u v0 = __builtin_bit_cast(v2u, trread(vb));
    v2u v1 = __builtin_bit_cast(v2u, trread(vb + 512));
    v2u sk = *(LAS v2u*)(kvw + 5120 + 8 * lane);
    if (zero_halo) { k0.x = 0; k0.y = 0; }
    float kk[12] = {bflo(k0.x), bfhi(k0.x), bflo(k0.y), bfhi(k0.y), bflo(k1.x), bfhi(k1.x), bflo(k1.y), bfhi(k1.y),
                    bflo(k2.x), bfhi(k2.x), bflo(k2.y), bfhi(k2.y)};
    float vv[8] = {bflo(v0.x), bfhi(v0.x), bflo(v0.y), bfhi(v0.y), bflo(v1.x), bfhi(v1.x), bflo(v1.y), bfhi(v1.y)};
    const float w0 = bflo(sk.x), w1 = bfhi(sk.x), w2 = bflo(sk.y), w3 = bfhi(sk.y);
    float ks[8];
#pragma unroll
    for (int i = 0; i < 8; ++i) ks[i] = kk[i + 1] * w3 + kk[i + 2] * w2 + kk[i + 3] * w1 + kk[i + 4] * w0;
    float u[8];
#pragma unroll
    for (int i = 0; i < 8; i += 2) {
        unsigned r = pk2(ks[i], ks[i + 1]);
        u[i] = bflo(r) * vv[i];
        u[i + 1] = bfhi(r) * vv[i + 1];
    }
    return (v4u){pk2(u[7], u[6]), pk2(u[5], u[4]), pk2(u[3], u[2]), pk2(u[1], u[0])};
}

__device__ __forceinline__ unsigned ab16(unsigned hi, unsigned lo) { return __builtin_amdgcn_alignbit(hi, lo, 16); }
__device__ __forceinline__ void fill_u(LAS char* sm, v2u ua, v4u ubv, int ch, int L) {
    if (L == 0) { ua.x = 0; ua.y = 0; }
    const unsigned w0 = ua.x, w1 = ua.y, w2 = ubv.x, w3 = ubv.y, w4 = ubv.z, w5 = ubv.w;
    const unsigned t0 = ab16(w1, w0), t1 = ab16(w2, w1), t2 = ab16(w3, w2), t3 = ab16(w4, w3), t4 = ab16(w5, w4);
    LAS char* ub = sm + L_U + ch * U_CH;
    LAS char* up = ub + 16 * L + 64;
    *(LAS v4u*)(up) = (v4u){t1, t2, t3, t4};
    *(LAS v4u*)(up + U_CP) = (v4u){w1, w2, w3, w4};
    *(LAS v4u*)(up + 2 * U_CP) = (v4u){t0, t1, t2, t3};
    *(LAS v4u*)(up + 3 * U_CP) = (v4u){w0, w1, w2, w3};
    if (L == 255) {
        LAS char* e = ub + 16 * 256 + 64;
        *(LAS v4u*)(e) = (v4u){w5 >> 16, 0, 0, 0};
        *(LAS v4u*)(e + U_CP) = (v4u){w5, 0, 0, 0};
        *(LAS v4u*)(e + 2 * U_CP) = (v4u){t4, w5 >> 16, 0, 0};
        *(LAS v4u*)(e + 3 * U_CP) = (v4u){w4, w5, 0, 0};
    }
}
__device__ __forceinline__ void fill_h(LAS char* sm, v4u ha, v2u hbv, int ch, int L) {
    if (L == 255) { hbv.x = 0; hbv.y = 0; }
    LAS char* hb = sm + L_H + ch * H_CH;
    LAS char* hp = hb + ((L >> 3) + 8) * 160 + (L & 7) * 16;
    *(LAS v4u*)(hp) = ha;
    *(LAS v4u*)(hp + H_CP) = (v4u){ha.z, ha.w, hbv.x, hbv.y};
    if (L == 0) *(LAS v4u*)(hb + H_CP + 7 * 160 + 7 * 16) = (v4u){0, 0, ha.x, ha.y};
}
__device__ __forceinline__ void fill_pads_h(LAS char* sm, int ch, int half, int lane) {
    const v4u z = {0, 0, 0, 0};
    LAS char* hb = sm + L_H + ch * H_CH;
    if (!(half == 1 && lane == 63)) *(LAS v4u*)(hb + half * H_CP + (lane >> 3) * 160 + (lane & 7) * 16) = z;
}
__device__ __forceinline__ void fill_pads_u(LAS char* sm, int ch, int half, int lane) {
    const v4u z = {0, 0, 0, 0};
    if (lane < 16) {
        LAS char* ub = sm + L_U + ch * U_CH;
        const int c = 2 * half + (lane >> 3), jx = lane & 7;
        if (jx != 4) *(LAS v4u*)(ub + c * U_CP + (jx < 4 ? 16 * jx : 16 * (252 + jx) + 64)) = z;
    }
}


template <int I> struct IC { static constexpr int v = I; };
template <int A, int B, class Fn> __device__ __forceinline__ void static_for(Fn&& fn) {
    if constexpr (A < B) { fn(IC<A>{}); static_for<A + 1, B>(fn); }
}
template <int NTA, int NTB, class Hook>
__device__ __forceinline__ void conv_body(LAS char* ua, LAS char* hb, f32x4& aA0, f32x4& aA1, f32x4& aB0, f32x4& aB1, Hook hook) {
    constexpr int NA = 16 * NTA + 17, NB = 16 * NTB + 17, T0 = NA - NB;
    constexpr int HK = 8;
    LAS char* up = ua - 64 * (NA - 1);
    asm volatile("" : "+v"(up), "+v"(hb));
    s16x8 W[NA + 1], BA[NA], BB[NB];
#define HOFF(t) (((t) & 1) ? (((t) + 1) / 2) * 160 : ((t) / 2) * 160 + 64)
    W[0] = *(LAS s16x8*)up;
    constexpr int PF = 3;
    static_for<0, PF>([&](auto nc) __attribute__((always_inline)) {
        constexpr int nl = decltype(nc)::v;
        W[nl + 1] = *(LAS s16x8*)(up + 64 * (nl + 1));
        BA[nl] = *(LAS s16x8*)(hb + HOFF(nl));
    });
    SCHED();
    static_for<0, NA>([&](auto tc) __attribute__((always_inline)) {
        constexpr int t = decltype(tc)::v, nl = t + PF;
        aA1 = MFMA(W[t], BA[t], aA1);
        SCHED();
        if constexpr (nl + 1 < NA) W[nl + 1] = *(LAS s16x8*)(up + 64 * (nl + 1));
        SCHED();
        if constexpr (t + 1 < NA) aA0 = MFMA(W[t + 1], BA[t], aA0);
        SCHED();
        if constexpr (nl < NA) BA[nl] = *(LAS s16x8*)(hb + HOFF(nl));
        SCHED();
        if constexpr (t >= T0) { aB1 = MFMA(W[t], BB[t - T0], aB1); SCHED(); }
        if constexpr (nl >= T0 && nl < NA) { BB[nl - T0] = *(LAS s16x8*)(hb + HOFF(nl - T0)); SCHED(); }
        if constexpr (t >= T0 && t + 1 < NA) { aB0 = MFMA(W[t + 1], BB[t - T0], aB0); SCHED(); }
        if constexpr (t == HK) { hook(); SCHED(); }
    });
}

__global__ __launch_bounds__(512, 2) void fused(const u16* __restrict__ q, const u16* __restrict__ kin, const u16* __restrict__ vin,
                                                const u16* __restrict__ sk, const u16* __restrict__ hk, const u16* __restrict__ Dv,
                                                const u16* __restrict__ w1, const u16* __restrict__ w2, const u16* __restrict__ gw,
                                                char* __restrict__ ws, u16* __restrict__ out) {
    __shared__ __attribute__((aligned(16))) char smem[LDS_BYTES];
    LAS char* sm = (LAS char*)smem;
    const int b = blockIdx.x, x = b & 7, idx = b >> 3;
    const int tid = threadIdx.x, lane = tid & 63;
    const int w = __builtin_amdgcn_readfirstlane(tid >> 6);
    const int j = w & 3;
    const rsrc_t rs = __builtin_amdgcn_make_buffer_rsrc(ws, 0, WS_BYTES, 0x00020000);
    unsigned long long pc; unsigned len;
    asm volatile(".Lfused_pc:\n\ts_getpc_b64 %0\n\ts_mov_b32 %1, .Lfused_end-.Lfused_pc" : "=s"(pc), "=s"(len));
    unsigned ctch;
    {
        unsigned co = (unsigned)(idx * 8 + w) * 128u;
        co = co < len ? co : 0u;
        asm volatile("global_load_dword %0, %1, %2" : "=v"(ctch) : "v"(co), "s"(pc) : "memory");
    }
    asm volatile("" :: "s"(q), "s"(kin), "s"(vin), "s"(sk), "s"(hk), "s"(Dv), "s"(w1), "s"(w2), "s"(gw), "s"(ws), "s"(out));
    const int rt = 4 * x + (idx & 3), ct = idx >> 2;
    const int d0 = 64 * x + 2 * idx;
    const int c16 = lane & 15, qd = lane >> 4, qq = c16 >> 2, p4 = lane & 3;
    const bool isG = idx < 28;
    const int rp = 4 * (x >> 1) + (idx & 3), fp = 7 * (x & 1) + (idx >> 2);
    const int ch = j >> 1, half = j & 1;
    const int L0 = 128 * half + lane, L1 = L0 + 64;

#define PLACE(i) (((i) >> 1) * 32768 + j * 8192 + ((i) & 1) * 4096)
    const int w2rd = (4 * qd + qq) * 128 + 32 * ((2 * qd + (qq >> 1)) & 3) + 8 * p4;
#define W2READ(dst, addr)                                                                    \
    do {                                                                                     \
        _Pragma("unroll") for (int n_ = 0; n_ < 4; ++n_) {                                   \
            LAS char* a_ = sm + (addr) + (w2rd ^ (n_ << 5));                                 \
            dst[n_] = cat8(trread(a_), trread(a_ + 2048));                                   \
        }                                                                                    \
    } while (0)
    const unsigned w2off = (unsigned)((lane >> 3) * D + ((((((lane & 7) >> 1) ^ qd) << 1) | (lane & 1)) * 8)) * 2u;
    const u16* w2C = w2 + (size_t)(224 * j) * D + 64 * ct;
#define W2STAGE(base, k_, addr) glds16x4(base + (32 * (k_)) * D, w2off, w2off + 7168u, w2off + 14336u, w2off + 21504u, sm + (addr))
    const int h_off = WS_HF + (rt * 56 + 7 * w) * 4096 + lane * 16;
    const int es = 16 * j + c16, ed = 32 * (w >> 2) + 8 * qd;
    unsigned tag;
    v4u qv;
#define HLOAD(k_) do { _Pragma("unroll") for (int i_ = 0; i_ < 4; ++i_) hbuf[k_][i_] = ld128(rs, h_off + (k_) * 4096 + i_ * 1024); SCHED(); } while (0)
#define G2MMA(wf, k_) do { _Pragma("unroll") for (int n_ = 0; n_ < 4; ++n_) _Pragma("unroll") for (int s_ = 0; s_ < 4; ++s_) \
        acc2[n_][s_] = MFMA(wf[n_], __builtin_bit_cast(s16x8, hbuf[k_][s_]), acc2[n_][s_]); SCHED(); } while (0)

    unsigned ev = 0, dvr = 0, tch = 0;
    LAS char* kvw = sm + L_KV + j * 5632;
    if (w < 4) {
        const int r_ = 64 * idx + 16 * j - 8 + (lane >> 3);
        const unsigned co_ = (unsigned)(64 * x + (lane & 7) * 8) * 2u;
        if (lane >= 32) glds16(kin, (unsigned)((r_ < 0 ? 0 : r_) * D) * 2u + co_, kvw);
        const unsigned o1_ = (unsigned)((r_ + 8) * D) * 2u + co_;
        glds16(kin, o1_, kvw + 1024);
        glds16(vin, o1_, kvw + 3072);
        if (lane < 32) glds16(sk, (unsigned)(256 * x + lane * 8) * 2u, kvw + 5120);
        glds16(kin + 8 * D, o1_, kvw + 2048);
        glds16(vin + 8 * D, o1_, kvw + 4096);
        asm volatile("global_load_dword %0, %2, %3 sc1\n\tglobal_load_ushort %1, %2, %4" : "=&v"(ev), "=&v"(dvr) : "v"(0), "s"(ws + WS_E), "s"(Dv + d0 + ch) : "memory");
        const int fpc = fp < 14 ? fp : 13, l5 = (lane & 31) + 32 * (j & 1);
        const int id = 128 + fpc * 64 + l5;
        const unsigned to = j < 2 ? (unsigned)((rp * 128 + (id & 127)) * 1024 + (id >> 7) * 128) : (unsigned)((32 * rp + (l5 >> 1)) * (F * 2) + fpc * 256 + (l5 & 1) * 128);
        asm volatile("global_load_dword %0, %1, %2" : "=v"(tch) : "v"(to), "s"(j < 2 ? (const char*)q : (const char*)w1) : "memory");
    }
    SCHED();

    if (w < 4) {
        unsigned dvs;
        if (w == 0 && lane < 32 && lane != F_TAG) *(LAS unsigned*)(sm + L_FL + 4 * lane) = 0u;
        const int sh = j & 1, fh = j >> 1;
        LAS char* bad[2][2]; LAS char* aad[2][4];
        {
            const int key = qq | ((qd & 1) << 2);
#pragma unroll
            for (int sp = 0; sp < 2; ++sp) {
                bad[sp][0] = sm + sp * 65536 + (64 * sh + c16) * 128 + 16 * (qd ^ (c16 >> 1));
                bad[sp][1] = sm + sp * 65536 + (64 * sh + c16) * 128 + 16 * ((4 + qd) ^ (c16 >> 1));
#pragma unroll
                for (int ft = 0; ft < 4; ++ft) aad[sp][ft] = sm + sp * 65536 + 16384 + (8 * qd + qq) * 256 + 32 * ((4 * fh + ft) ^ key) + 8 * p4;
                asm volatile("" : "+v"(bad[sp][0]), "+v"(bad[sp][1]), "+v"(aad[sp][0]), "+v"(aad[sp][1]), "+v"(aad[sp][2]), "+v"(aad[sp][3]));
            }
        }
        LAS char* tsrc = sm + L_T + (16 * j + (lane >> 3)) * 144 + (lane & 7) * 16;
        int tdst = WS_UT + (64 * x + 16 * j + (lane >> 3)) * 4096 + (1984 - 64 * idx) * 2 + (lane & 7) * 16;
        asm volatile("" : "+v"(tsrc), "+v"(tdst));
        WAIT_V(5);
        *(LAS v4u*)(sm + L_T + lane * 144 + (7 - 2 * j) * 16) = kv_prep(kvw, 0, lane, idx == 0 && j == 0);
        SCHED(); WAIT_V(3);
        *(LAS v4u*)(sm + L_T + lane * 144 + (6 - 2 * j) * 16) = kv_prep(kvw, 1, lane, false);
        WAIT_L0(); SCHED();
        BAR();
#define UT_STORE() do { v4u o0_ = *(LAS v4u*)tsrc, o1_ = *(LAS v4u*)(tsrc + 8 * 144); st128(o0_, rs, tdst); st128(o1_, rs, tdst + 8 * 4096); } while (0)
#define GET_TAG() do { \
            asm volatile("s_waitcnt vmcnt(0)\n\tv_readfirstlane_b32 %0, %2\n\tv_readfirstlane_b32 %1, %3\n\ts_nop 3" : "=s"(tag), "=s"(dvs) : "v"(ev), "v"(dvr), "v"(tch), "v"(ctch) : "memory"); \
            tag = tag * 0x9E3779B1u + 0x7F4A7C15u; \
            if (tid == 0) *(LAS unsigned*)(sm + L_FL + 4 * F_TAG) = tag; } while (0)
        const int fu_st = WS_FU + (x * 32 + idx) * 128;
        if (isG) {
            f32x4 acc[4][4] = {};
            s16x8 fa[2][2][4], fb[2][2][4];
#define READ(c)                                                                                     \
    do {                                                                                            \
        _Pragma("unroll") for (int kk = 0; kk < 2; ++kk) {                                          \
            _Pragma("unroll") for (int ft = 0; ft < 4; ++ft) {                                      \
                fb[(c) & 1][kk][ft] = *(LAS s16x8*)(bad[((c) >> 1) & 1][kk] + ((c) & 1) * 32768 + 2048 * ft); \
                LAS char* a_ = aad[((c) >> 1) & 1][ft] + ((c) & 1) * 32768 + kk * 8192;             \
                fa[(c) & 1][kk][ft] = cat8(trread(a_), trread(a_ + 1024));                          \
            }                                                                                       \
        }                                                                                           \
    } while (0)
#define MMA(c)                                                                                      \
    do {                                                                                            \
        _Pragma("unroll") for (int kk = 0; kk < 2; ++kk)                                            \
            _Pragma("unroll") for (int ft = 0; ft < 4; ++ft)                                        \
                _Pragma("unroll") for (int st = 0; st < 4; ++st)                                    \
                    acc[ft][st] = MFMA(fa[(c) & 1][kk][ft], fb[(c) & 1][kk][st], acc[ft][st]);      \
    } while (0)
#define ILV() do { _Pragma("unroll") for (int g_ = 0; g_ < 24; ++g_) { __builtin_amdgcn_sched_group_barrier(0x100, 1, 0); __builtin_amdgcn_sched_group_barrier(0x8, 1, 0); } \
                   __builtin_amdgcn_sched_group_barrier(0x8, 8, 0); SCHED(); } while (0)
            SCHED(); UT_STORE(); READ(0); SCHED(); WAIT_L0();
            BAR(); SCHED(); READ(1); MMA(0); ILV(); WAIT_L0();
            BAR(); SCHED(); READ(2); MMA(1); ILV(); WAIT_L0();
            BAR(); SCHED(); READ(3); MMA(2); ILV();
            GET_TAG(); WAIT_L0();
            BAR();
            if (tid == 0) st32(tag, rs, fu_st);
            SCHED(); READ(4); MMA(3); ILV(); WAIT_L0();
            BAR(); SCHED(); READ(5); MMA(4); ILV(); WAIT_L0();
            BAR(); SCHED(); READ(6); MMA(5); ILV(); WAIT_L0();
            BAR(); SCHED();
            READ(7); MMA(6); ILV(); WAIT_L0();
            lds_set(sm, F_C7 + j, lane);
            SCHED();
            MMA(7);
            const int hst = WS_HF + ((2 * rp + sh) * 56 + 4 * fp + 2 * fh) * 4096 + lane * 16;
#pragma unroll
            for (int pp = 0; pp < 2; ++pp)
#pragma unroll
                for (int st = 0; st < 4; ++st) st128(silu8(acc[2 * pp][st], acc[2 * pp + 1][st]), rs, hst + pp * 4096 + st * 1024);
        } else {
            UT_STORE();
            GET_TAG(); WAIT_L0();
            BAR();
            if (tid == 0) st32(tag, rs, fu_st);
            const int r0 = 512 * (x >> 1) + 64 * (4 * (x & 1) + (idx - 28)) + 16 * j;
            v4u gv = *(const GAS v4u*)(gw + lane * 8);
            float gs[2];
#pragma unroll 1
            for (int hh = 0; hh < 2; ++hh) {
                v4u qr[8];
#pragma unroll
                for (int i = 0; i < 8; ++i) qr[i] = *(const GAS v4u*)(q + (size_t)(r0 + 8 * hh + i) * D + lane * 8);
                float s[8];
#pragma unroll
                for (int i = 0; i < 8; ++i) {
                    s[i] = 0.f;
#pragma unroll
                    for (int e = 0; e < 4; ++e) {
                        unsigned r = pk2(bflo(qr[i][e]) * bflo(gv[e]), bfhi(qr[i][e]) * bfhi(gv[e]));
                        s[i] += bflo(r) + bfhi(r);
                    }
                }
#pragma unroll
                for (int i = 0; i < 4; ++i) { const bool hi = lane & 32; float snd = hi ? s[i] : s[i + 4], kp = hi ? s[i + 4] : s[i]; s[i] = kp + __shfl_xor(snd, 32); }
#pragma unroll
                for (int i = 0; i < 2; ++i) { const bool hi = lane & 16; float snd = hi ? s[i] : s[i + 2], kp = hi ? s[i + 2] : s[i]; s[i] = kp + __shfl_xor(snd, 16); }
                { const bool hi = lane & 8; float snd = hi ? s[0] : s[1], kp = hi ? s[1] : s[0]; s[0] = kp + __shfl_xor(snd, 8); }
                s[0] += __shfl_xor(s[0], 4); s[0] += __shfl_xor(s[0], 2); s[0] += __shfl_xor(s[0], 1);
                if (hh == 0) gs[0] = s[0]; else gs[1] = s[0];
            }
            if ((lane & 7) < 2) {
                float t = rbf((lane & 1) ? gs[1] : gs[0]);
                float g = rbf(__builtin_amdgcn_rcpf(1.0f + __builtin_amdgcn_exp2f(-1.44269504088896f * t)));
                st64((v2u){__float_as_uint(g), tag}, rs, WS_G + (r0 + 8 * (lane & 1) + (lane >> 3)) * 8);
            }
        }
#define H_PUBLISH() do { WAIT_V(0); if (isG && lane == 0) st32(tag, rs, WS_FH + (rp * 16 + fp) * 128 + j * 4); } while (0)
        qv = *(const GAS v4u*)(q + (size_t)(64 * rt + es) * D + 64 * ct + ed);
        s16x8 wl[7][4];
        lds_wait(sm, F_C01 + j, 0, lane);
        W2READ(wl[0], PLACE(0)); W2READ(wl[1], PLACE(1));
        const bool fill_ready = __all(*(volatile LAS unsigned*)(sm + L_FL + 4 * (F_FILL + 2 * ch + (lane & 1))) != 0u);
        WAIT_L0(); SCHED();
        lds_set(sm, F_PART + j, lane);
        if (!fill_ready) {
            H_PUBLISH();
            lds_wait(sm, F_FILL + 2 * ch, 1, lane);
        }
        CBAR();
        __builtin_amdgcn_s_setprio(3);
        f32x4 aA0 = {}, aA1 = {}, aB0 = {}, aB1 = {};
        const int b0 = c16 >> 3, b1 = c16 & 7;
        v2u uu[2][2];
        {
            LAS char* u3 = sm + L_U + ch * U_CH + 3 * U_CP + 64 + 2 * (2048 - (8 * qd + 4 * b0 + 64 * b1));
#pragma unroll
            for (int t = 0; t < 2; ++t)
#pragma unroll
                for (int mt = 0; mt < 2; ++mt) uu[t][mt] = *(LAS v2u*)(u3 - 1024 * (t ? half : 3 - half) - 64 * mt);
        }
        {
            LAS char* ua_ = sm + L_U + ch * U_CH + (c16 & 3) * U_CP + 64 + 4096 + 16 * (qd - (c16 >> 2));
            LAS char* hb_ = sm + L_H + ch * H_CH + (c16 >> 3) * H_CP + (c16 & 7) * 160 + qd * 16;
            auto hook = [&]() __attribute__((always_inline)) { if (fill_ready) H_PUBLISH(); };
            if (half == 0) conv_body<3, 0>(ua_, hb_, aA0, aA1, aB0, aB1, hook);
            else conv_body<2, 1>(ua_, hb_, aA0, aA1, aB0, aB1, hook);
        }
        v4u hbuf[7][4];
        f32x4 acc2[4][4] = {};
        asm volatile("" : "+v"(uu[0][0]), "+v"(uu[0][1]), "+v"(uu[1][0]), "+v"(uu[1][1]));
        lds_set(sm, F_CONV + j, lane);
        {
            const float dd = bflo(dvs);
            const int srcl = ((lane >> 3) + 8 * ((lane >> 2) & 1) + 16 * (lane & 3)) * 4;
#pragma unroll
            for (int t = 0; t < 2; ++t) {
                v2u zz[2];
#pragma unroll
                for (int mt = 0; mt < 2; ++mt) {
                    f32x4 y = t ? (mt ? aB1 : aB0) : (mt ? aA1 : aA0);
                    v2u u_ = uu[t][mt];
                    unsigned y01 = pk2(y[0], y[1]), y23 = pk2(y[2], y[3]);
                    unsigned p01 = pk2(bfhi(u_.y) * dd, bflo(u_.y) * dd), p23 = pk2(bfhi(u_.x) * dd, bflo(u_.x) * dd);
                    zz[mt].x = pk2(bflo(y01) + bflo(p01), bfhi(y01) + bfhi(p01));
                    zz[mt].y = pk2(bflo(y23) + bflo(p23), bfhi(y23) + bfhi(p23));
                }
                v2u snd = b0 ? zz[0] : zz[1], keep = b0 ? zz[1] : zz[0], rcv;
                rcv.x = __builtin_amdgcn_update_dpp(0, (int)snd.x, 0x128, 0xF, 0xF, false);
                rcv.y = __builtin_amdgcn_update_dpp(0, (int)snd.y, 0x128, 0xF, 0xF, false);
                v4u o = b0 ? (v4u){rcv.x, rcv.y, keep.x, keep.y} : (v4u){keep.x, keep.y, rcv.x, rcv.y};
#pragma unroll
                for (int e = 0; e < 4; ++e) o[e] = __builtin_amdgcn_ds_bpermute(srcl, o[e]);
                st128(o, rs, WS_Z + (d0 + ch) * 4096 + (t ? half : 3 - half) * 1024 + lane * 16);
            }
            WAIT_V(0);
            if (lane == 0) st32(tag, rs, WS_FZ + (x * 32 + idx) * 128 + j * 4);
            SCHED();
        }
        __builtin_amdgcn_s_setprio(0);
        lds_wait(sm, F_W2OK + j, 0, lane);
        HLOAD(0); HLOAD(1); HLOAD(2); HLOAD(3);
        G2MMA(wl[0], 0);
        HLOAD(4); HLOAD(5); HLOAD(6);
        SCHED();
        W2READ(wl[2], PLACE(2)); SCHED(); G2MMA(wl[1], 1);
        W2READ(wl[3], PLACE(3)); SCHED(); G2MMA(wl[2], 2);
        W2READ(wl[4], PLACE(5)); SCHED(); G2MMA(wl[3], 3);
        W2READ(wl[5], PLACE(0)); SCHED(); G2MMA(wl[4], 4);
        W2READ(wl[6], PLACE(1)); SCHED(); G2MMA(wl[5], 5);
        G2MMA(wl[6], 6);
#pragma unroll
        for (int s = 0; s < 4; ++s) {
            LAS char* pp = sm + PLACE(s) + c16 * 256;
#pragma unroll
            for (int n = 0; n < 4; ++n) *(LAS f32x4*)(pp + (((4 * n + qd) ^ c16) << 4)) = acc2[n][s];
        }
    } else {
        const int fu_off = WS_FU + (x * 32 + (lane & 31)) * 128;
        const u16* w2L = w2 + (size_t)(224 * w) * D + 64 * ct;
        const unsigned xs0 = (unsigned)((rp * 128 + 32 * j + (lane >> 3)) * D + (((lane & 7) ^ qd) * 8)) * 2u;
        const unsigned xs1 = (unsigned)((rp * 128 + 32 * j + (lane >> 3)) * D + (((lane & 7) ^ (4 + qd)) * 8)) * 2u + 7168u;
        const unsigned ws0 = (unsigned)((16 * j + qd) * F + fp * 128 + (((((c16 >> 1) ^ qd) << 1) | (lane & 1)) * 8)) * 2u;
        const unsigned ws1 = (unsigned)((16 * j + qd) * F + fp * 128 + (((((c16 >> 1) ^ (qd | 4)) << 1) | (lane & 1)) * 8)) * 2u + 2u * 13312u;
#define STAGE(c)                                                                                   \
    do {                                                                                           \
        LAS char* sl = sm + ((c) & 3) * 32768 + 4096 * j;                                          \
        glds16x4(q + (c) * 64, xs0, xs1, xs0 + 14336u, xs1 + 14336u, sl);                          \
        glds16x4(w1 + (c) * 64 * F, ws0, ws0 + 13312u, ws1, ws1 + 13312u, sl + 16384);             \
    } while (0)
        const char* hrow = (const char*)hk + (size_t)(d0 + ch) * 4096;
        const int urow = WS_UT + (d0 + ch) * 4096;
        v4u ha0, ha1, ub0, ub1; v2u hb0, hb1, ua0, ua1;
#define HKLOAD() asm volatile("global_load_dwordx4 %0, %4, %6 nt\n\tglobal_load_dwordx2 %1, %4, %6 offset:16 nt\n\t" \
                     "global_load_dwordx4 %2, %4, %6 offset:1024 nt\n\tglobal_load_dwordx2 %3, %5, %6 nt" \
                     : "=&v"(ha0), "=&v"(hb0), "=&v"(ha1), "=&v"(hb1) \
                     : "v"(16 * L0), "v"(L1 == 255 ? 0 : 16 * L1 + 16), "s"(hrow) : "memory")
        if (isG) {
            STAGE(0); STAGE(1);
            asm volatile("s_waitcnt vmcnt(8)" :: "v"(ctch) : "memory"); BAR(); STAGE(2);
            WAIT_V(8); BAR(); STAGE(3);
            WAIT_V(8); BAR(); STAGE(4);
            WAIT_V(8); BAR(); STAGE(5);
            WAIT_V(8); BAR(); STAGE(6); STAGE(7);
        } else {
            unsigned t2_, t3_;
            const unsigned wo = (unsigned)((((idx - 28) * 8 + x) * 4 + j) * 112 + lane) * 128u;
            BAR();
            BAR();
            asm volatile("global_load_dword %0, %2, %4\n\tglobal_load_dword %1, %3, %4\n\ts_waitcnt vmcnt(0)"
                         : "=&v"(t2_), "=&v"(t3_) : "v"(wo), "v"(wo + (lane < 48 ? 8192u : 0u)), "s"(w2), "v"(ctch) : "memory");
        }
        HKLOAD();
        tag = __builtin_amdgcn_readfirstlane(*(volatile LAS unsigned*)(sm + L_FL + 4 * F_TAG));
        if (isG) { WAIT_V(20); BAR(); }
        if (isG) { WAIT_V(12); BAR(); }
        W2STAGE(w2C, 0, PLACE(0)); W2STAGE(w2C, 1, PLACE(1));
        if (isG) { WAIT_V(12); BAR(); }
        unsigned pr = 0;
        if (j == 0) asm volatile("global_load_dword %0, %1, %2 sc1" : "=v"(pr) : "v"(fu_off), "s"(ws) : "memory");
        W2STAGE(w2L, 0, PLACE(2)); W2STAGE(w2L, 1, PLACE(3));
        asm volatile("s_waitcnt vmcnt(16)" : "+v"(ha0), "+v"(hb0), "+v"(ha1), "+v"(hb1) :: "memory");
        __builtin_amdgcn_s_setprio(2);
        fill_pads_h(sm, ch, half, lane);
        fill_h(sm, ha0, hb0, ch, L0); fill_h(sm, ha1, hb1, ch, L1);
        SCHED();
        asm volatile("s_waitcnt vmcnt(8)" : "+v"(pr) :: "memory");
        lds_set(sm, F_C01 + j, lane);
        if (j == 0) {
            if (!__all(pr == tag)) poll_flags(rs, fu_off, tag);
            lds_set(sm, F_UT, lane);
        }
        lds_wait(sm, F_UT, 0, lane);
        asm volatile("global_load_dwordx2 %0, %4, %6 sc1\n\tglobal_load_dwordx4 %1, %5, %6 sc1\n\t"
                     "global_load_dwordx2 %2, %5, %6 offset:1016 sc1\n\tglobal_load_dwordx4 %3, %5, %6 offset:1024 sc1"
                     : "=&v"(ua0), "=&v"(ub0), "=&v"(ua1), "=&v"(ub1)
                     : "v"(urow + (L0 == 0 ? 0 : 16 * L0 - 8)), "v"(urow + 16 * L0), "s"(ws) : "memory");
        __builtin_amdgcn_s_setprio(0);
        W2STAGE(w2L, 2, PLACE(4)); W2STAGE(w2L, 3, PLACE(5));
        __builtin_amdgcn_s_setprio(2);
        if (isG) lds_wait(sm, F_C7, 3, lane);
        asm volatile("s_waitcnt vmcnt(8)" : "+v"(ua0), "+v"(ub0), "+v"(ua1), "+v"(ub1) :: "memory");
        fill_pads_u(sm, ch, half, lane);
        fill_u(sm, ua0, ub0, ch, L0); fill_u(sm, ua1, ub1, ch, L1);
        WAIT_L0(); SCHED();
        lds_set(sm, F_FILL + j, lane);
        __builtin_amdgcn_s_setprio(0);
        s16x8 wr[6][4];
        WAIT_V(8);
        W2READ(wr[0], PLACE(2)); W2READ(wr[1], PLACE(3));
        WAIT_L0(); SCHED();
        W2STAGE(w2L, 4, PLACE(2)); W2STAGE(w2L, 5, PLACE(3));
        WAIT_V(8);
        W2READ(wr[2], PLACE(4)); W2READ(wr[3], PLACE(5));
        WAIT_L0(); SCHED();
        W2STAGE(w2L, 6, PLACE(4)); W2STAGE(w2C, 4, PLACE(5));
        lds_wait(sm, F_PART + j, 0, lane);
        W2STAGE(w2C, 5, PLACE(0)); W2STAGE(w2C, 6, PLACE(1));
        WAIT_V(16);
        W2READ(wr[4], PLACE(2)); W2READ(wr[5], PLACE(3));
        WAIT_L0(); SCHED();
        W2STAGE(w2C, 2, PLACE(2)); W2STAGE(w2C, 3, PLACE(3));
        if (b == 255 && w == 5) {
#pragma unroll 1
            for (unsigned spins = 0; spins < SPIN_CAP; ++spins) {
                CBAR();
                bool ok = true;
#pragma unroll
                for (int i = 0; i < 4; ++i) ok = (ld32(rs, WS_FU + (i * 64 + lane) * 128) == tag) && ok;
                if (__all(ok)) break;
                __builtin_amdgcn_s_sleep(2);
            }
            if (lane == 0) st32(tag, rs, WS_E);
        }
        {
            const int kpl = ((7 * (w - 4 * ((lane >> 2) & 1))) >> 1) + (lane & 3);
            poll_flags_at(ws, WS_FH + ((rt >> 1) * 16 + (kpl >> 1)) * 128 + ((rt & 1) + 2 * (kpl & 1)) * 4, tag, lane < 8);
            WAIT_V(0);
        }
        v4u hbuf[7][4];
        f32x4 acc2[4][4] = {};
        lds_set(sm, F_W2OK + j, lane);
        __builtin_amdgcn_s_setprio(2);
        HLOAD(0); HLOAD(1); HLOAD(2); HLOAD(3);
        G2MMA(wr[0], 0); HLOAD(4); HLOAD(5);
        G2MMA(wr[1], 1); HLOAD(6);
        qv = *(const GAS v4u*)(q + (size_t)(64 * rt + es) * D + 64 * ct + ed);
        SCHED();
        s16x8 wl[4];
        G2MMA(wr[2], 2);
        G2MMA(wr[3], 3);
        const int fz_off = WS_FZ + (ct * 32 + 8 * j + (c16 >> 1)) * 128 + ((c16 & 1) * 2 + (((rt >> 3) == 3 || (rt >> 3) == 0) ? 0 : 1)) * 4;
        G2MMA(wr[4], 4);
        W2READ(wl, PLACE(4)); SCHED(); G2MMA(wr[5], 5);
        __builtin_amdgcn_s_waitcnt(0x0F70);
        unsigned pk = lane < 16 ? ~tag : tag;
        if (lane < 16) asm volatile("global_atomic_or %0, %1, %3, %2 sc0 sc1" : "+v"(pk) : "v"(fz_off), "s"(ws), "v"(0) : "memory");
        SCHED();
        G2MMA(wl, 6);
        __builtin_amdgcn_s_setprio(0);
        lds_wait(sm, F_CONV, 3, lane);
#pragma unroll
        for (int s = 0; s < 4; ++s) {
            LAS char* pp = sm + (s < 3 ? L_CV + j * 16384 + s * 4096 : PLACE(4)) + c16 * 256;
#pragma unroll
            for (int n = 0; n < 4; ++n) *(LAS f32x4*)(pp + (((4 * n + qd) ^ c16) << 4)) = acc2[n][s];
        }
        SCHED();
        asm volatile("s_waitcnt vmcnt(0)" : "+v"(pk) :: "memory");
        if (__any(pk != tag)) poll_flags_at(ws, fz_off, tag, pk != tag);
        CBAR();
        const int zoff = WS_Z + (64 * ct + 16 * j + (lane >> 3)) * 4096 + rt * 128 + (lane & 7) * 16;
        v4u z0 = ld128(rs, zoff), z1 = ld128(rs, zoff + 8 * 4096);
        v2u gr;
#pragma unroll 1
        for (unsigned spins = 0; spins < SPIN_CAP; ++spins) {
            CBAR();
            gr = ld64(rs, WS_G + (64 * rt + lane) * 8);
            if (__all(gr.y == tag)) break;
            __builtin_amdgcn_s_sleep(6);
        }
        *(LAS v4u*)(sm + L_CV + j * 16384 + 12288 + lane * 16) = z0;
        *(LAS v4u*)(sm + L_CV + j * 16384 + 12288 + 1024 + lane * 16) = z1;
        if (j == 0) *(LAS unsigned*)(sm + L_CV + 12288 + 2048 + lane * 4) = gr.x;
    }
    WAIT_L0();
    BAR();
    {
        const int zr = ed + qq;
        LAS char* zp = sm + L_CV + 12288 + (zr >> 4) * 16384 + (zr & 15) * 128 + 32 * j + 8 * p4;
        v2u za = __builtin_bit_cast(v2u, trread(zp)), zb = __builtin_bit_cast(v2u, trread(zp + 512));
        const float gg = *(LAS float*)(sm + L_CV + 12288 + 2048 + es * 4);
        const int sl0 = ((ed >> 2) ^ c16) << 4, sl1 = (((ed >> 2) + 1) ^ c16) << 4;
        LAS char* pp = sm + (j >> 1) * 32768 + (j & 1) * 4096 + c16 * 256;
        LAS char* pl = sm + (j < 3 ? L_CV + j * 4096 : 65536) + c16 * 256;
        const int lstr = j < 3 ? 16384 : 8192;
        f32x4 s0 = *(LAS f32x4*)(pp + sl0), s1 = *(LAS f32x4*)(pp + sl1);
#pragma unroll
        for (int i = 1; i < 4; ++i) { s0 += *(LAS f32x4*)(pp + i * 8192 + sl0); s1 += *(LAS f32x4*)(pp + i * 8192 + sl1); }
#pragma unroll
        for (int i = 0; i < 4; ++i) { s0 += *(LAS f32x4*)(pl + i * lstr + sl0); s1 += *(LAS f32x4*)(pl + i * lstr + sl1); }
        const float g1 = rbf(1.0f - gg);
        unsigned zw[4] = {za.x, za.y, zb.x, zb.y};
        float bs[8] = {s0[0], s0[1], s0[2], s0[3], s1[0], s1[1], s1[2], s1[3]};
        v4u o;
#pragma unroll
        for (int e = 0; e < 4; ++e) {
            unsigned bb = pk2(bs[2 * e], bs[2 * e + 1]);
            unsigned aa = pk2(bflo(qv[e]) * bflo(zw[e]), bfhi(qv[e]) * bfhi(zw[e]));
            unsigned ga = pk2(gg * bflo(aa), gg * bfhi(aa));
            unsigned gb = pk2(g1 * bflo(bb), g1 * bfhi(bb));
            o[e] = pk2(bflo(ga) + bflo(gb), bfhi(ga) + bfhi(gb));
        }
        const rsrc_t ro = __builtin_amdgcn_make_buffer_rsrc(out, 0, S * D * 2, 0x00020000);
        __builtin_amdgcn_raw_buffer_store_b128(o, ro, ((64 * rt + es) * D + 64 * ct + ed) * 2, 0, 17);
    }
    asm volatile(".Lfused_end:" ::: "memory");
}

extern "C" void kernel_launch(void* const* d_in, const int* in_sizes, int n_in, void* d_out, int out_size, void* d_ws, size_t ws_size,
                              hipStream_t stream) {
    const u16* q = (const u16*)d_in[0]; const u16* k = (const u16*)d_in[1]; const u16* v = (const u16*)d_in[2];
    const u16* sk = (const u16*)d_in[3]; const u16* hk = (const u16*)d_in[4]; const u16* Dv = (const u16*)d_in[5];
    const u16* w1 = (const u16*)d_in[7]; const u16* w2 = (const u16*)d_in[8]; const u16* gw = (const u16*)d_in[9];
    fused<<<256, 512, 0, stream>>>(q, k, v, sk, hk, Dv, w1, w2, gw, (char*)d_ws, (u16*)d_out);
}
```
